```python
import torch

def _softmax(x, axis=-1):
    return torch.softmax(x, dim=axis)

def _gelu(x):
    return torch.nn.functional.gelu(x, approximate='tanh')

def _silu(x):
    return torch.nn.functional.silu(x)

def _sigmoid(x):
    return torch.sigmoid(x)

def _layernorm(x, axis=-1, eps=1e-05):
    m = x.mean(dim=axis, keepdim=True)
    v = x.var(dim=axis, keepdim=True, unbiased=False)
    return (x - m) / torch.sqrt(v + eps)

def _scatter_add(tgt, idx, src, axis=0):
    return tgt.clone().index_add_(axis, idx, src)

def _segment_sum(src, seg, n):
    out = torch.zeros((n,) + src.shape[1:], dtype=src.dtype)
    return out.index_add_(0, seg, src)

def _pad(x, pw):
    flat = tuple((v for pair in reversed(pw) for v in pair))
    return torch.nn.functional.pad(x, flat)
kernel_name = 'sequential_preLN__memory_aug_recurrent_mem_tr__glu_gated_geglu__prompt'

def setup_inputs():

    _w = lambda a: (lambda t: t.to(torch.bfloat16) if t.is_floating_point() else t)(torch.as_tensor(a))

    def _A_setup():
        B, S, Dm = (1, 2048, 640)
        H, Dh = (32, 192)
        M, seg = (8, 32)
        seg = min(seg, S)
        S = S // seg * seg
        return {'x': _w((lambda *s: torch.randn(*s, dtype=torch.bfloat16) * 0.05)(B, S, Dm)), 'mem0': _w((lambda *s: torch.randn(*s, dtype=torch.bfloat16) * 0.2)(B, M, Dm)), 'Wq': _w((lambda *s: torch.randn(*s, dtype=torch.bfloat16) * 1.6)(Dm, H, Dh) * Dm ** (-0.5)), 'Wk': _w((lambda *s: torch.randn(*s, dtype=torch.bfloat16) * 1.6)(Dm, H, Dh) * Dm ** (-0.5)), 'Wv': _w((lambda *s: torch.randn(*s, dtype=torch.bfloat16) * 0.2)(Dm, H, Dh) * Dm ** (-0.5)), 'Wo': _w((lambda *s: torch.randn(*s, dtype=torch.bfloat16) * 0.2)(H, Dh, Dm) * (H * Dh) ** (-0.5)), 'seg_len': seg}
    _A_in = _A_setup()

    def _B_setup():
        B, S, Dm, Df = (1, 2048, 640, 1536)
        return {'x': _w((lambda *s: torch.randn(*s, dtype=torch.bfloat16) * 0.05)(B, S, Dm)), 'w_gate': _w((lambda *s: torch.randn(*s, dtype=torch.bfloat16) * 0.4)(Dm, Df) * Dm ** (-0.5)), 'w_up': _w((lambda *s: torch.randn(*s, dtype=torch.bfloat16) * 0.4)(Dm, Df) * Dm ** (-0.5)), 'w_down': _w((lambda *s: torch.randn(*s, dtype=torch.bfloat16) * 0.4)(Df, Dm) * Df ** (-0.5))}
    _B_in = _B_setup()
    return {'A_x': _A_in['x'], 'A_mem0': _A_in['mem0'], 'A_Wq': _A_in['Wq'], 'A_Wk': _A_in['Wk'], 'A_Wv': _A_in['Wv'], 'A_Wo': _A_in['Wo'], 'A_seg_len': _A_in['seg_len'], 'B_x': _B_in['x'], 'B_w_gate': _B_in['w_gate'], 'B_w_up': _B_in['w_up'], 'B_w_down': _B_in['w_down']}

def reference(A_x, A_mem0, A_Wq, A_Wk, A_Wv, A_Wo, A_seg_len, B_x, B_w_gate, B_w_up, B_w_down):

    def _A_ref(x, mem0, Wq, Wk, Wv, Wo, seg_len):
        B, S, Dm = x.shape
        H, Dh = (Wq.shape[1], Wq.shape[2])
        M = mem0.shape[1]
        scale = Dh ** (-0.5)
        n_seg = S // seg_len
        L = M + seg_len
        idx = torch.arange(L)
        mask = (idx[:, None] < M) | (idx[:, None] >= idx[None, :])
        mem = mem0
        outs = []
        for s in range(n_seg):
            xs = x[:, s * seg_len:(s + 1) * seg_len, :].clone()
            h = torch.concatenate([mem, xs], axis=1)
            q = torch.einsum('btd,dhe->bhte', h, Wq)
            k = torch.einsum('btd,dhe->bhte', h, Wk)
            v = torch.einsum('btd,dhe->bhte', h, Wv)
            scores = torch.einsum('bhqe,bhke->bhqk', q, k) * scale
            scores = torch.where(mask[None, None], scores, -1000000000.0)
            p = _softmax(scores, axis=-1)
            o = torch.einsum('bhqk,bhke->bhqe', p, v)
            o = torch.einsum('bhte,hed->btd', o, Wo)
            mem = o[:, :M, :].clone()
            outs.append(o[:, M:, :])
        return torch.concatenate(outs, axis=1)

    def _B_ref(x, w_gate, w_up, w_down):
        g = torch.einsum('bsd,df->bsf', x, w_gate)
        u = torch.einsum('bsd,df->bsf', x, w_up)
        h = _gelu(g) * u
        return torch.einsum('bsf,fd->bsd', h, w_down)
    x = A_x
    h = x + _A_ref(_layernorm(x, axis=-1), A_mem0, A_Wq, A_Wk, A_Wv, A_Wo, A_seg_len)
    h2 = h + _B_ref(_layernorm(h, axis=-1), B_w_gate, B_w_up, B_w_down)
    return h2
```

```cpp
#include <hip/hip_runtime.h>
#include <hip/hip_bf16.h>
#include <stdint.h>

namespace nv {
typedef unsigned short u16;
__device__ __forceinline__ float bf2f(u16 v) { return __uint_as_float(((unsigned)v) << 16); }
__device__ __forceinline__ u16 f2bf(float f) { __hip_bfloat16 b = __float2bfloat16(f); return *(u16*)&b; }
__device__ __forceinline__ float rbf(float f) { return bf2f(f2bf(f)); }

constexpr int S = 2048, DM = 640, H = 32, DH = 192, HD = H * DH, M = 8, SEG = 32, L = 40, NSEG = 64, DF = 1536;

__global__ void k_ln(const u16* __restrict__ x, u16* __restrict__ y) {
  __shared__ float red[256];
  int row = blockIdx.x, t = threadIdx.x;
  float v[3]; float s = 0;
  for (int i = 0; i < 3; ++i) { int c = t + i * 256; v[i] = c < DM ? bf2f(x[row * DM + c]) : 0.f; s += v[i]; }
  red[t] = s; __syncthreads();
  for (int o = 128; o > 0; o >>= 1) { if (t < o) red[t] += red[t + o]; __syncthreads(); }
  float mean = red[0] / DM; __syncthreads();
  float q = 0;
  for (int i = 0; i < 3; ++i) { int c = t + i * 256; if (c < DM) { float d = v[i] - mean; q += d * d; } }
  red[t] = q; __syncthreads();
  for (int o = 128; o > 0; o >>= 1) { if (t < o) red[t] += red[t + o]; __syncthreads(); }
  float var = red[0] / DM;
  float mb = rbf(mean), vb = rbf(var);
  float sd = rbf(sqrtf(rbf(vb + 1e-5f)));
  for (int i = 0; i < 3; ++i) { int c = t + i * 256; if (c < DM) y[row * DM + c] = f2bf(rbf(v[i] - mb) / sd); }
}

__global__ void k_gemm(const u16* __restrict__ A, int lda, const u16* __restrict__ B, int ldb, u16* __restrict__ C, int ldc, int Mm, int N, int K) {
  int n = blockIdx.x * 64 + (threadIdx.x & 63), m = blockIdx.y * 4 + (threadIdx.x >> 6);
  if (m >= Mm || n >= N) return;
  float acc = 0;
  for (int k = 0; k < K; ++k) acc += bf2f(A[(size_t)m * lda + k]) * bf2f(B[(size_t)k * ldb + n]);
  C[(size_t)m * ldc + n] = f2bf(acc);
}

__global__ void k_concat(const u16* __restrict__ mem, const u16* __restrict__ xs, u16* __restrict__ h) {
  int i = blockIdx.x * 256 + threadIdx.x;
  if (i >= L * DM) return;
  h[i] = i < M * DM ? mem[i] : xs[i - M * DM];
}

__global__ void k_attn(const u16* __restrict__ q, const u16* __restrict__ k, const u16* __restrict__ v, u16* __restrict__ o) {
  __shared__ float p[L][L];
  int h = blockIdx.x, t = threadIdx.x;
  const float scale = 0.07216878364870322f;
  for (int idx = t; idx < L * L; idx += blockDim.x) {
    int i = idx / L, j = idx % L;
    float acc = 0;
    for (int e = 0; e < DH; ++e) acc += bf2f(q[i * HD + h * DH + e]) * bf2f(k[j * HD + h * DH + e]);
    float sc = rbf(rbf(acc) * scale);
    bool ok = (i < M) || (i >= j);
    p[i][j] = ok ? sc : rbf(-1e9f);
  }
  __syncthreads();
  if (t < L) {
    float mx = -3e38f; for (int j = 0; j < L; ++j) mx = fmaxf(mx, p[t][j]);
    float sum = 0; for (int j = 0; j < L; ++j) { float e = expf(p[t][j] - mx); p[t][j] = e; sum += e; }
    for (int j = 0; j < L; ++j) p[t][j] = rbf(p[t][j] / sum);
  }
  __syncthreads();
  for (int idx = t; idx < L * DH; idx += blockDim.x) {
    int i = idx / DH, e = idx % DH;
    float acc = 0;
    for (int j = 0; j < L; ++j) acc += p[i][j] * bf2f(v[j * HD + h * DH + e]);
    o[i * HD + h * DH + e] = f2bf(acc);
  }
}

__global__ void k_split(const u16* __restrict__ oo, u16* __restrict__ memnext, const u16* __restrict__ x, u16* __restrict__ hres) {
  int i = blockIdx.x * 256 + threadIdx.x;
  if (i >= L * DM) return;
  if (i < M * DM) memnext[i] = oo[i];
  else hres[i - M * DM] = f2bf(bf2f(x[i - M * DM]) + bf2f(oo[i]));
}

__global__ void k_geglu(const u16* __restrict__ g, const u16* __restrict__ u, u16* __restrict__ a, int n) {
  int i = blockIdx.x * 256 + threadIdx.x; if (i >= n) return;
  float x = bf2f(g[i]);
  float t = tanhf(0.7978845608028654f * (x + 0.044715f * x * x * x));
  float ge = rbf(0.5f * x * (1.f + t));
  a[i] = f2bf(ge * bf2f(u[i]));
}
__global__ void k_add(const u16* __restrict__ a, const u16* __restrict__ b, u16* __restrict__ c, int n) {
  int i = blockIdx.x * 256 + threadIdx.x; if (i >= n) return;
  c[i] = f2bf(bf2f(a[i]) + bf2f(b[i]));
}

static void run(void* const* d_in, void* d_out, void* d_ws, hipStream_t st) {
  const u16* x = (const u16*)d_in[0]; const u16* mem0 = (const u16*)d_in[1];
  const u16* Wq = (const u16*)d_in[2]; const u16* Wk = (const u16*)d_in[3]; const u16* Wv = (const u16*)d_in[4]; const u16* Wo = (const u16*)d_in[5];
  const u16* wg = (const u16*)d_in[8]; const u16* wu = (const u16*)d_in[9]; const u16* wd = (const u16*)d_in[10];
  u16* ws = (u16*)d_ws;
  u16* memall = ws; ws += 65 * M * DM;
  u16* xn = ws; ws += S * DM;
  u16* hc = ws; ws += L * DM;
  u16* q = ws; ws += L * HD; u16* k = ws; ws += L * HD; u16* v = ws; ws += L * HD; u16* o = ws; ws += L * HD;
  u16* oo = ws; ws += L * DM;
  u16* hres = ws; ws += S * DM;
  u16* hn = ws; ws += S * DM;
  u16* g = ws; ws += S * DF; u16* u = ws; ws += S * DF; u16* a = ws; ws += S * DF;
  u16* f = ws; ws += S * DM;
  k_ln<<<S, 256, 0, st>>>(x, xn);
  hipMemcpyAsync(memall, mem0, M * DM * 2, hipMemcpyDeviceToDevice, st);
  for (int s = 0; s < NSEG; ++s) {
    k_concat<<<(L * DM + 255) / 256, 256, 0, st>>>(memall + s * M * DM, xn + s * SEG * DM, hc);
    dim3 gq(HD / 64, L / 4);
    k_gemm<<<gq, 256, 0, st>>>(hc, DM, Wq, HD, q, HD, L, HD, DM);
    k_gemm<<<gq, 256, 0, st>>>(hc, DM, Wk, HD, k, HD, L, HD, DM);
    k_gemm<<<gq, 256, 0, st>>>(hc, DM, Wv, HD, v, HD, L, HD, DM);
    k_attn<<<H, 256, 0, st>>>(q, k, v, o);
    dim3 go(DM / 64, L / 4);
    k_gemm<<<go, 256, 0, st>>>(o, HD, Wo, DM, oo, DM, L, DM, HD);
    k_split<<<(L * DM + 255) / 256, 256, 0, st>>>(oo, memall + (s + 1) * M * DM, x + s * SEG * DM, hres + s * SEG * DM);
  }
  k_ln<<<S, 256, 0, st>>>(hres, hn);
  dim3 gf(DF / 64, S / 4);
  k_gemm<<<gf, 256, 0, st>>>(hn, DM, wg, DF, g, DF, S, DF, DM);
  k_gemm<<<gf, 256, 0, st>>>(hn, DM, wu, DF, u, DF, S, DF, DM);
  k_geglu<<<(S * DF + 255) / 256, 256, 0, st>>>(g, u, a, S * DF);
  dim3 gd(DM / 64, S / 4);
  k_gemm<<<gd, 256, 0, st>>>(a, DF, wd, DM, f, DM, S, DM, DF);
  k_add<<<(S * DM + 255) / 256, 256, 0, st>>>(hres, f, (u16*)d_out, S * DM);
}
}

extern "C" void kernel_launch(void* const* d_in, const int* in_sizes, int n_in, void* d_out, int out_size, void* d_ws, size_t ws_size, hipStream_t stream) {
  nv::run(d_in, d_out, d_ws, stream);
}
```

```cpp
#include <hip/hip_runtime.h>
#include <hip/hip_bf16.h>
#include <stdint.h>

namespace rmt {
typedef unsigned short u16;
typedef unsigned v4u __attribute__((ext_vector_type(4)));
typedef unsigned v2u __attribute__((ext_vector_type(2)));
typedef float f32x4 __attribute__((ext_vector_type(4)));
typedef short bf16x8 __attribute__((ext_vector_type(8)));
typedef short bf16x4 __attribute__((ext_vector_type(4)));
typedef __bf16 bfv2 __attribute__((ext_vector_type(2)));
#define LAS __attribute__((address_space(3)))
#define GAS __attribute__((address_space(1)))

constexpr int S = 2048, DM = 640, H = 32, DH = 192, HD = H * DH, SEG = 32, NSEG = 64, DF = 1536;
constexpr int KS = DM / 32;
constexpr unsigned SENT = 0xFFFFFFFFu;
constexpr int AUX_SC1 = 16;

constexpr size_t OFF_MEM = 0;
constexpr size_t SZ_MEM = 65 * 10240;
constexpr size_t OFF_P = OFF_MEM + SZ_MEM;
constexpr size_t SZ_P = (size_t)64 * 20480;
constexpr size_t OFF_CTL = OFF_P + SZ_P;
constexpr size_t SZ_CTL = 1024;
constexpr size_t SZ_SENT = OFF_CTL + SZ_CTL;
constexpr size_t OFF_XNF = (SZ_SENT + 4095) / 4096 * 4096;
constexpr size_t OFF_WF = OFF_XNF + (size_t)128 * 20 * 1024;
constexpr size_t OFF_WVF = OFF_WF + (size_t)32 * 24 * 20 * 1024;
constexpr size_t OFF_WGU = OFF_WVF + (size_t)80 * 16 * 20 * 1024;
constexpr size_t OFF_WD = OFF_WGU + (size_t)96 * 2 * 20 * 1024;
constexpr size_t OFF_QX = OFF_WD + (size_t)40 * 48 * 1024;
constexpr size_t OFF_KX = OFF_QX + (size_t)64 * 32 * 12 * 1024;
constexpr size_t OFF_KM = OFF_KX + (size_t)64 * 32 * 12 * 1024;
constexpr size_t OFF_PX = OFF_KM + (size_t)64 * 32 * 6 * 1024;
constexpr size_t OFF_VO = OFF_PX + (size_t)64 * 16 * 4 * 1536;
constexpr size_t OFF_H = OFF_VO + (size_t)64 * 80 * 16 * 1280;
constexpr size_t OFF_WOF = OFF_H;
constexpr size_t OFF_HNF = OFF_H + (size_t)S * DM * 4;
constexpr size_t OFF_AF = OFF_HNF + (size_t)128 * 20 * 1024;
constexpr size_t WS_END = OFF_AF + (size_t)128 * 48 * 1024;
static_assert(OFF_WOF + (size_t)40 * 192 * 1024 <= WS_END, "WOF alias");
static_assert(WS_END <= (size_t)256 * 1024 * 1024, "workspace");

__device__ __forceinline__ float bf2f(unsigned v) { return __uint_as_float(v << 16); }
__device__ __forceinline__ float bflo(unsigned v) { return __uint_as_float(v << 16); }
__device__ __forceinline__ float bfhi(unsigned v) { return __uint_as_float(v & 0xFFFF0000u); }
__device__ __forceinline__ unsigned pack2(float a, float b) {
  bfv2 v = {(__bf16)a, (__bf16)b};
  return __builtin_bit_cast(unsigned, v);
}
__device__ __forceinline__ unsigned nosent(unsigned d) { return d == SENT ? 0x7FC07FC0u : d; }
__device__ __forceinline__ f32x4 mfma32(v4u a, v4u b, f32x4 c) {
  return __builtin_amdgcn_mfma_f32_16x16x32_bf16(__builtin_bit_cast(bf16x8, a), __builtin_bit_cast(bf16x8, b), c, 0, 0, 0);
}
__device__ __forceinline__ f32x4 mfma16(v2u a, v2u b, f32x4 c) {
  return __builtin_amdgcn_mfma_f32_16x16x16bf16_1k(__builtin_bit_cast(bf16x4, a), __builtin_bit_cast(bf16x4, b), c, 0, 0, 0);
}
__device__ __forceinline__ float xmax4(float x) {
  auto s = __builtin_amdgcn_permlane16_swap(__float_as_uint(x), __float_as_uint(x), false, false);
  x = fmaxf(__uint_as_float(s[0]), __uint_as_float(s[1]));
  auto t = __builtin_amdgcn_permlane32_swap(__float_as_uint(x), __float_as_uint(x), false, false);
  return fmaxf(__uint_as_float(t[0]), __uint_as_float(t[1]));
}
__device__ __forceinline__ float xsum4(float x) {
  auto s = __builtin_amdgcn_permlane16_swap(__float_as_uint(x), __float_as_uint(x), false, false);
  x = __uint_as_float(s[0]) + __uint_as_float(s[1]);
  auto t = __builtin_amdgcn_permlane32_swap(__float_as_uint(x), __float_as_uint(x), false, false);
  return __uint_as_float(t[0]) + __uint_as_float(t[1]);
}
__device__ __forceinline__ bool valid4(v4u v) { return (v.x != SENT) & (v.y != SENT) & (v.z != SENT) & (v.w != SENT); }
__device__ __forceinline__ bool valid2(v2u v) { return (v.x != SENT) & (v.y != SENT); }

constexpr int NB_WQ = 20 * 48, NB_WO = 192 * 5, NB_WG = 20 * 12, NB_WD = 48 * 5;
constexpr int PT_BLOCKS = 2 * NB_WQ + NB_WO + 2 * NB_WG + NB_WD;
constexpr int LN_BLOCKS = S / 16;

__device__ __forceinline__ void ln_rows_bf16(const u16* __restrict__ x, unsigned char* __restrict__ outF, int blk) {
  const int t = threadIdx.x, lane = t & 63, w = t >> 6;
  const int row = blk * 16 + w * 4 + (lane >> 4), q = lane & 15;
  float v[5][8];
  float s = 0.f;
#pragma unroll
  for (int i = 0; i < 5; ++i) {
    v4u d = *(const v4u*)(x + (size_t)row * DM + (q + 16 * i) * 8);
    v[i][0] = bflo(d.x); v[i][1] = bfhi(d.x); v[i][2] = bflo(d.y); v[i][3] = bfhi(d.y);
    v[i][4] = bflo(d.z); v[i][5] = bfhi(d.z); v[i][6] = bflo(d.w); v[i][7] = bfhi(d.w);
#pragma unroll
    for (int j = 0; j < 8; ++j) s += v[i][j];
  }
#pragma unroll
  for (int o = 1; o < 16; o <<= 1) s += __shfl_xor(s, o);
  const float mean = s * (1.f / DM);
  float qq = 0.f;
#pragma unroll
  for (int i = 0; i < 5; ++i)
#pragma unroll
    for (int j = 0; j < 8; ++j) { float d = v[i][j] - mean; qq += d * d; }
#pragma unroll
  for (int o = 1; o < 16; o <<= 1) qq += __shfl_xor(qq, o);
  const float rs = 1.f / sqrtf(qq * (1.f / DM) + 1e-5f);
#pragma unroll
  for (int i = 0; i < 5; ++i) {
    const int pc = q + 16 * i, kk = pc >> 2, g = pc & 3;
    v4u o;
    o.x = pack2((v[i][0] - mean) * rs, (v[i][1] - mean) * rs);
    o.y = pack2((v[i][2] - mean) * rs, (v[i][3] - mean) * rs);
    o.z = pack2((v[i][4] - mean) * rs, (v[i][5] - mean) * rs);
    o.w = pack2((v[i][6] - mean) * rs, (v[i][7] - mean) * rs);
    *(v4u*)(outF + ((size_t)(row >> 4) * 20 + kk) * 1024 + (g * 16 + (row & 15)) * 16) = o;
  }
}

__global__ void __launch_bounds__(256) k_pre(const u16* __restrict__ x, const u16* __restrict__ mem0, const u16* __restrict__ Wq,
                                             const u16* __restrict__ Wk, const u16* __restrict__ Wo, const u16* __restrict__ wg,
                                             const u16* __restrict__ wu, const u16* __restrict__ wd, unsigned char* __restrict__ ws) {
  __shared__ unsigned tile[32 * 65];
  int b = blockIdx.x;
  const int t = threadIdx.x;
  if (b >= PT_BLOCKS) {
    b -= PT_BLOCKS;
    if (b < LN_BLOCKS) { ln_rows_bf16(x, ws + OFF_XNF, b); return; }
    for (int pc = t; pc < 640; pc += 256) {
      const int c8 = pc >> 3, i = pc & 7;
      v4u d = *(const v4u*)(mem0 + i * DM + c8 * 8);
      d.x = nosent(d.x); d.y = nosent(d.y); d.z = nosent(d.z); d.w = nosent(d.w);
      *(v4u*)(ws + OFF_MEM + pc * 16) = d;
    }
    return;
  }
  const u16* src; int ld, kk, nb, job;
  if (b < NB_WQ) { job = 0; src = Wq; ld = HD; kk = b / 48; nb = b % 48; }
  else if (b < 2 * NB_WQ) { b -= NB_WQ; job = 1; src = Wk; ld = HD; kk = b / 48; nb = b % 48; }
  else if (b < 2 * NB_WQ + NB_WO) { b -= 2 * NB_WQ; job = 2; src = Wo; ld = DM; kk = b / 5; nb = b % 5; }
  else if (b < 2 * NB_WQ + NB_WO + NB_WG) { b -= 2 * NB_WQ + NB_WO; job = 3; src = wg; ld = DF; kk = b / 12; nb = b % 12; }
  else if (b < 2 * NB_WQ + NB_WO + 2 * NB_WG) { b -= 2 * NB_WQ + NB_WO + NB_WG; job = 4; src = wu; ld = DF; kk = b / 12; nb = b % 12; }
  else { b -= 2 * NB_WQ + NB_WO + 2 * NB_WG; job = 5; src = wd; ld = DM; kk = b / 5; nb = b % 5; }
  {
    const int row = t >> 3, ch = t & 7;
    const v4u* p = (const v4u*)(src + (size_t)(kk * 32 + row) * ld + nb * 128 + ch * 16);
    v4u a = p[0], c = p[1];
    unsigned* d = tile + row * 65 + ch * 8;
    d[0] = a.x; d[1] = a.y; d[2] = a.z; d[3] = a.w; d[4] = c.x; d[5] = c.y; d[6] = c.z; d[7] = c.w;
  }
  __syncthreads();
  const u16* tl = (const u16*)tile;
#pragma unroll
  for (int it = 0; it < 2; ++it) {
    const int pc = t + it * 256, i = pc >> 6, lane = pc & 63, g = lane >> 4, n = lane & 15;
    unsigned e[8];
#pragma unroll
    for (int j = 0; j < 8; ++j) {
      const int row = (job == 5) ? (16 * (j >> 2) + 4 * g + (j & 3)) : (8 * g + j);
      e[j] = tl[row * 130 + i * 16 + n];
    }
    v4u o = {e[0] | (e[1] << 16), e[2] | (e[3] << 16), e[4] | (e[5] << 16), e[6] | (e[7] << 16)};
    const int nt = nb * 8 + i;
    size_t dst;
    if (job <= 1) { const int h = nt / 12, et = nt % 12; dst = OFF_WF + ((size_t)(h * 24 + job * 12 + et) * 20 + kk) * 1024; }
    else if (job == 2) dst = OFF_WOF + ((size_t)nt * 192 + kk) * 1024;
    else if (job <= 4) dst = OFF_WGU + ((size_t)(nt * 2 + (job - 3)) * 20 + kk) * 1024;
    else dst = OFF_WD + ((size_t)nt * 48 + kk) * 1024;
    *(v4u*)(ws + dst + lane * 16) = o;
  }
}

__global__ void __launch_bounds__(256) k_wvo(const u16* __restrict__ Wv, unsigned char* __restrict__ ws) {
  const int lane = threadIdx.x & 63, g = lane >> 4, n = lane & 15;
  const int wv = blockIdx.x * 4 + (threadIdx.x >> 6);
  const int h = wv / 40, rem = wv % 40, mg = rem >> 2, cq = rem & 3;
  v4u a[4][6];
#pragma unroll
  for (int m = 0; m < 4; ++m)
#pragma unroll
    for (int ke = 0; ke < 6; ++ke)
      a[m][ke] = *(const v4u*)(Wv + (size_t)((mg * 4 + m) * 16 + n) * HD + h * DH + ke * 32 + g * 8);
  for (int ci = 0; ci < 10; ++ci) {
    const int ct = cq * 10 + ci;
    v4u bf[6];
#pragma unroll
    for (int ke = 0; ke < 6; ++ke) bf[ke] = *(const v4u*)(ws + OFF_WOF + ((size_t)ct * 192 + h * 6 + ke) * 1024 + lane * 16);
#pragma unroll
    for (int m = 0; m < 4; ++m) {
      f32x4 acc = {0.f, 0.f, 0.f, 0.f};
#pragma unroll
      for (int ke = 0; ke < 6; ++ke) acc = mfma32(a[m][ke], bf[ke], acc);
      const int mt = mg * 4 + m;
      const int c8 = 2 * ct + (n >> 3), hp = h >> 1, kk = mt >> 1, g2 = 2 * (mt & 1) + (g >> 1), n2 = (h & 1) * 8 + (n & 7);
      v2u o = {pack2(acc[0], acc[1]), pack2(acc[2], acc[3])};
      *(v2u*)(ws + OFF_WVF + (((size_t)(c8 * 16 + hp) * 20 + kk) * 64 + g2 * 16 + n2) * 16 + (g & 1) * 8) = o;
    }
  }
}

__global__ void __launch_bounds__(256) k_qkx(unsigned char* __restrict__ ws) {
  const int lane = threadIdx.x & 63;
  const int bw = blockIdx.x;
  const int sg = bw & 15, wj = bw >> 4;
  const int s = sg * 4 + (threadIdx.x >> 6);
  const int h = wj >> 2, which = (wj >> 1) & 1, half = wj & 1;
  const unsigned char* wsrc = ws + OFF_WF + ((size_t)(h * 24 + which * 12 + half * 6) * 20) * 1024 + lane * 16;
  const unsigned char* xsrc = ws + OFF_XNF + ((size_t)(s * 2) * 20) * 1024 + lane * 16;
  f32x4 acc[6][2];
#pragma unroll
  for (int i = 0; i < 6; ++i) { acc[i][0] = f32x4{0, 0, 0, 0}; acc[i][1] = f32x4{0, 0, 0, 0}; }
#pragma unroll 2
  for (int kk = 0; kk < KS; ++kk) {
    v4u b0 = *(const v4u*)(xsrc + kk * 1024), b1 = *(const v4u*)(xsrc + (20 + kk) * 1024);
#pragma unroll
    for (int i = 0; i < 6; ++i) {
      v4u a = *(const v4u*)(wsrc + (i * 20 + kk) * 1024);
      acc[i][0] = mfma32(a, b0, acc[i][0]);
      acc[i][1] = mfma32(a, b1, acc[i][1]);
    }
  }
  const float sc = which == 0 ? 0.07216878364870322f * 1.4426950408889634f : 1.f;
  unsigned char* dst = ws + (which == 0 ? OFF_QX : OFF_KX) + ((size_t)(s * 32 + h) * 12) * 1024 + lane * 16;
#pragma unroll
  for (int rt = 0; rt < 2; ++rt)
#pragma unroll
    for (int p = 0; p < 3; ++p) {
      f32x4 lo = acc[2 * p][rt], hi = acc[2 * p + 1][rt];
      v4u o = {pack2(lo[0] * sc, lo[1] * sc), pack2(lo[2] * sc, lo[3] * sc), pack2(hi[0] * sc, hi[1] * sc), pack2(hi[2] * sc, hi[3] * sc)};
      *(v4u*)(dst + (rt * 6 + half * 3 + p) * 1024) = o;
    }
}

constexpr int NSC = 32, NCOL = 80, CH_T = 512;
constexpr unsigned SPIN_CAP = 1u << 22;
constexpr int SL_W = 0, SL_MEM = 96 * 1024, SL_QK = SL_MEM + 10240, SL_KX = SL_QK + 12 * 1024, SL_END = SL_KX + 24 * 1024;
constexpr int CL_X = 0, CL_MEM = 80 * 1024, CL_PART = CL_MEM + 10240, CL_END = CL_PART + 4096;
constexpr int CH_LDS = SL_END > CL_END ? SL_END : CL_END;
static_assert(CH_LDS <= 160 * 1024, "lds");

__device__ __forceinline__ v4u ldsr4(LAS unsigned char* p) { return *(LAS v4u*)p; }

__device__ __forceinline__ bool gather_mem(__amdgpu_buffer_rsrc_t rs, int s, int w, int lane, LAS unsigned char* img, GAS unsigned* ctl) {
  const int p0 = 80 * w + lane, p1 = 80 * w + 64 + (lane & 15);
  const int base = (int)OFF_MEM + s * 10240;
  v4u v0, v1;
  for (unsigned spins = 0;; ++spins) {
    asm volatile("" ::: "memory");
    v0 = __builtin_amdgcn_raw_buffer_load_b128(rs, base + p0 * 16, 0, AUX_SC1);
    v1 = __builtin_amdgcn_raw_buffer_load_b128(rs, base + p1 * 16, 0, AUX_SC1);
    if (__all(valid4(v0) & valid4(v1))) break;
    if (spins > SPIN_CAP) { __hip_atomic_store(ctl, 1u, __ATOMIC_RELAXED, __HIP_MEMORY_SCOPE_AGENT); return false; }
  }
  *(LAS v4u*)(img + p0 * 16) = v0;
  if (lane < 16) *(LAS v4u*)(img + p1 * 16) = v1;
  return true;
}

__device__ __forceinline__ void score_role(unsigned char* __restrict__ ws, int h, LAS unsigned char* L) {
  const int tid = threadIdx.x, lane = tid & 63, g = lane >> 4, n = lane & 15;
  const int w = __builtin_amdgcn_readfirstlane(tid >> 6);
  const __amdgpu_buffer_rsrc_t rs = __builtin_amdgcn_make_buffer_rsrc((void*)ws, 0, (int)SZ_SENT, 0x00020000);
  GAS unsigned* ctl = (GAS unsigned*)(ws + OFF_CTL);
  v4u wr[48];
  {
    const unsigned char* src = ws + OFF_WF + ((size_t)(h * 24 + 3 * w) * 20) * 1024 + lane * 16;
#pragma unroll
    for (int t = 0; t < 3; ++t)
#pragma unroll
      for (int kk = 0; kk < 16; ++kk) wr[t * 16 + kk] = *(const v4u*)(src + (t * 20 + kk) * 1024);
#pragma unroll
    for (int t = 0; t < 3; ++t)
#pragma unroll
      for (int kk = 16; kk < 20; ++kk)
        *(LAS v4u*)(L + SL_W + (w * 12 + t * 4 + (kk - 16)) * 1024 + lane * 16) = *(const v4u*)(src + (t * 20 + kk) * 1024);
  }
  {
    const unsigned char* src = ws + OFF_KX + ((size_t)(0 * 32 + h) * 12) * 1024 + lane * 16;
    *(LAS v4u*)(L + SL_KX + w * 1024 + lane * 16) = *(const v4u*)(src + w * 1024);
    if (w < 4) *(LAS v4u*)(L + SL_KX + (8 + w) * 1024 + lane * 16) = *(const v4u*)(src + (8 + w) * 1024);
  }
  const float qs = w < 4 ? 0.07216878364870322f * 1.4426950408889634f : 1.f;
  LAS unsigned char* memrd = L + SL_MEM + (g * 8 + (n & 7)) * 16;
  LAS unsigned char* wl = L + SL_W + (w * 12) * 1024 + lane * 16;
  for (int s = 0; s < NSEG - 1; ++s) {
    if (!gather_mem(rs, s, w, lane, L + SL_MEM, ctl)) return;
    __syncthreads();
    f32x4 acc[3] = {{0, 0, 0, 0}, {0, 0, 0, 0}, {0, 0, 0, 0}};
#pragma unroll
    for (int kk = 0; kk < KS; ++kk) {
      const v4u b = ldsr4(memrd + kk * 512);
#pragma unroll
      for (int t = 0; t < 3; ++t) {
        const v4u a = kk < 16 ? wr[t * 16 + kk] : ldsr4(wl + (t * 4 + kk - 16) * 1024);
        acc[t] = mfma32(a, b, acc[t]);
      }
    }
#pragma unroll
    for (int t = 0; t < 3; ++t) {
      const int tau = 3 * w + t;
      v2u o = {pack2(acc[t][0] * qs, acc[t][1] * qs), pack2(acc[t][2] * qs, acc[t][3] * qs)};
      *(LAS v2u*)(L + SL_QK + (tau >> 1) * 1024 + lane * 16 + (tau & 1) * 8) = o;
    }
    __syncthreads();
    if (w == 0) {
      LAS unsigned char* kx = L + SL_KX + (s & 1) * 12288 + lane * 16;
      LAS unsigned char* qk = L + SL_QK + lane * 16;
      f32x4 sm = {0, 0, 0, 0}, s0 = {0, 0, 0, 0}, s1 = {0, 0, 0, 0};
#pragma unroll
      for (int p = 0; p < 6; ++p) {
        const v4u q = ldsr4(qk + p * 1024), km = ldsr4(qk + (6 + p) * 1024);
        const v4u k0 = ldsr4(kx + p * 1024), k1 = ldsr4(kx + (6 + p) * 1024);
        sm = mfma32(km, q, sm);
        s0 = mfma32(k0, q, s0);
        s1 = mfma32(k1, q, s1);
      }
      const float NEG = -3.0e38f;
      if (g >= 2) sm = f32x4{NEG, NEG, NEG, NEG};
      float mx = fmaxf(fmaxf(fmaxf(sm[0], sm[1]), fmaxf(sm[2], sm[3])), fmaxf(fmaxf(fmaxf(s0[0], s0[1]), fmaxf(s0[2], s0[3])), fmaxf(fmaxf(s1[0], s1[1]), fmaxf(s1[2], s1[3]))));
      mx = xmax4(mx);
      float em[4], e0[4], e1[4], sum = 0.f;
#pragma unroll
      for (int r = 0; r < 4; ++r) {
        em[r] = __builtin_amdgcn_exp2f(sm[r] - mx); e0[r] = __builtin_amdgcn_exp2f(s0[r] - mx); e1[r] = __builtin_amdgcn_exp2f(s1[r] - mx);
        sum += em[r] + e0[r] + e1[r];
      }
      sum = xsum4(sum);
      const float inv = 1.f / sum;
      v4u px = {nosent(pack2(e0[0] * inv, e0[1] * inv)), nosent(pack2(e0[2] * inv, e0[3] * inv)),
                nosent(pack2(e1[0] * inv, e1[1] * inv)), nosent(pack2(e1[2] * inv, e1[3] * inv))};
      v2u pm = {nosent(pack2(em[0] * inv, em[1] * inv)), nosent(pack2(em[2] * inv, em[3] * inv))};
      const int pb = (int)OFF_P + s * 20480 + h * 640;
      if (n < 8) {
        __builtin_amdgcn_raw_buffer_store_b128(px, rs, pb + (g * 8 + n) * 16, 0, AUX_SC1);
        if (g < 2) __builtin_amdgcn_raw_buffer_store_b64(pm, rs, pb + 512 + (g * 8 + n) * 8, 0, AUX_SC1);
      }
    }
    {
      const unsigned char* src = ws + OFF_KX + ((size_t)((s + 1) * 32 + h) * 12) * 1024 + lane * 16;
      LAS unsigned char* dst = L + SL_KX + ((s + 1) & 1) * 12288 + lane * 16;
      *(LAS v4u*)(dst + w * 1024) = *(const v4u*)(src + w * 1024);
      if (w < 4) *(LAS v4u*)(dst + (8 + w) * 1024) = *(const v4u*)(src + (8 + w) * 1024);
    }
  }
}

__device__ __forceinline__ void col_role(unsigned char* __restrict__ ws, int c8, LAS unsigned char* L) {
  const int tid = threadIdx.x, lane = tid & 63, g = lane >> 4, n = lane & 15;
  const int w = __builtin_amdgcn_readfirstlane(tid >> 6);
  const __amdgpu_buffer_rsrc_t rs = __builtin_amdgcn_make_buffer_rsrc((void*)ws, 0, (int)SZ_SENT, 0x00020000);
  GAS unsigned* ctl = (GAS unsigned*)(ws + OFF_CTL);
  v4u wr[40];
  {
    const unsigned char* src = ws + OFF_WVF + ((size_t)(c8 * 16 + 2 * w) * 20) * 1024 + lane * 16;
#pragma unroll
    for (int i = 0; i < 40; ++i) wr[i] = *(const v4u*)(src + i * 1024);
  }
  auto stage_x = [&](int seg) {
    const unsigned char* src = ws + OFF_XNF + (size_t)seg * 40960 + w * 5120 + lane * 16;
    LAS unsigned char* dst = L + CL_X + (seg & 1) * 40960 + w * 5120;
#pragma unroll
    for (int i = 0; i < 5; ++i)
      __builtin_amdgcn_global_load_lds((const GAS void*)(src + i * 1024), (LAS void*)(dst + i * 1024), 16, 0, 0);
  };
  stage_x(0);
  stage_x(1);
  __syncthreads();
  v4u vox[2]; v2u vom[2];
  auto compute_vox = [&](int seg) {
    LAS unsigned char* xr = L + CL_X + (seg & 1) * 40960 + lane * 16;
    f32x4 a00 = {0, 0, 0, 0}, a01 = {0, 0, 0, 0}, a10 = {0, 0, 0, 0}, a11 = {0, 0, 0, 0};
#pragma unroll
    for (int kk = 0; kk < KS; ++kk) {
      const v4u x0 = ldsr4(xr + kk * 1024), x1 = ldsr4(xr + (20 + kk) * 1024);
      a00 = mfma32(x0, wr[kk], a00); a01 = mfma32(x1, wr[kk], a01);
      a10 = mfma32(x0, wr[20 + kk], a10); a11 = mfma32(x1, wr[20 + kk], a11);
    }
    vox[0] = v4u{pack2(a00[0], a00[1]), pack2(a00[2], a00[3]), pack2(a01[0], a01[1]), pack2(a01[2], a01[3])};
    vox[1] = v4u{pack2(a10[0], a10[1]), pack2(a10[2], a10[3]), pack2(a11[0], a11[1]), pack2(a11[2], a11[3])};
  };
  compute_vox(0);
  LAS unsigned char* memrd = L + CL_MEM + (g * 8 + (n & 7)) * 16;
  for (int s = 0; s < NSEG; ++s) {
    if (!gather_mem(rs, s, w, lane, L + CL_MEM, ctl)) return;
    __syncthreads();
    {
      f32x4 m0 = {0, 0, 0, 0}, m1 = {0, 0, 0, 0};
#pragma unroll
      for (int kk = 0; kk < KS; ++kk) {
        const v4u a = ldsr4(memrd + kk * 512);
        m0 = mfma32(a, wr[kk], m0); m1 = mfma32(a, wr[20 + kk], m1);
      }
      vom[0] = v2u{pack2(m0[0], m0[1]), pack2(m0[2], m0[3])};
      vom[1] = v2u{pack2(m1[0], m1[1]), pack2(m1[2], m1[3])};
    }
    {
      unsigned char* dst = ws + OFF_VO + ((size_t)(s * 80 + c8) * 16 + 2 * w) * 1280;
#pragma unroll
      for (int tt = 0; tt < 2; ++tt) {
        *(v4u*)(dst + tt * 1280 + lane * 16) = vox[tt];
        if (g < 2) *(v2u*)(dst + tt * 1280 + 1024 + lane * 8) = vom[tt];
      }
    }
    if (s == NSEG - 1) break;
    v4u pa, pb; v2u pmv;
    {
      const int base = (int)OFF_P + s * 20480;
      const int oa = base + (4 * w + (n >> 3)) * 640 + (g * 8 + (n & 7)) * 16;
      const int om = base + (4 * w + 2 * (g >> 1) + (n >> 3)) * 640 + 512 + ((g & 1) * 8 + (n & 7)) * 8;
      for (unsigned spins = 0;; ++spins) {
        asm volatile("" ::: "memory");
        pa = __builtin_amdgcn_raw_buffer_load_b128(rs, oa, 0, AUX_SC1);
        pb = __builtin_amdgcn_raw_buffer_load_b128(rs, oa + 1280, 0, AUX_SC1);
        pmv = __builtin_amdgcn_raw_buffer_load_b64(rs, om, 0, AUX_SC1);
        if (__all(valid4(pa) & valid4(pb) & valid2(pmv))) break;
        if (spins > SPIN_CAP) { __hip_atomic_store(ctl, 1u, __ATOMIC_RELAXED, __HIP_MEMORY_SCOPE_AGENT); return; }
      }
    }
    {
      const v2u z = {0u, 0u};
      const v2u pm0 = g < 2 ? pmv : z, pm1 = g < 2 ? z : pmv;
      f32x4 d0 = {0, 0, 0, 0}, d1 = {0, 0, 0, 0};
      d0 = mfma32(vox[0], pa, d0); d1 = mfma32(vox[1], pb, d1);
      d0 = mfma16(vom[0], pm0, d0); d1 = mfma16(vom[1], pm1, d1);
      d0 += d1;
      if ((g >> 1) == (n >> 3)) *(LAS f32x4*)(L + CL_PART + w * 512 + (n >> 3) * 256 + (n & 7) * 32 + (g & 1) * 16) = d0;
    }
    __syncthreads();
    if (w == 0) {
      LAS unsigned char* pr = L + CL_PART + g * 1024 + n * 16;
      f32x4 t = *(LAS f32x4*)(pr) + *(LAS f32x4*)(pr + 256) + *(LAS f32x4*)(pr + 512) + *(LAS f32x4*)(pr + 768);
      t[0] = xsum4(t[0]); t[1] = xsum4(t[1]); t[2] = xsum4(t[2]); t[3] = xsum4(t[3]);
      v2u o = {nosent(pack2(t[0], t[1])), nosent(pack2(t[2], t[3]))};
      if (g == 0) __builtin_amdgcn_raw_buffer_store_b64(o, rs, (int)OFF_MEM + (s + 1) * 10240 + c8 * 128 + n * 8, 0, AUX_SC1);
    }
    compute_vox(s + 1);
    if (s + 2 < NSEG) stage_x(s + 2);
  }
}

__global__ void __launch_bounds__(CH_T, 2) k_chain(unsigned char* __restrict__ ws) {
  extern __shared__ __attribute__((aligned(16))) unsigned char smem[];
  LAS unsigned char* L = (LAS unsigned char*)smem;
  const int b = blockIdx.x;
  if (b < NSC) score_role(ws, b, L);
  else col_role(ws, b - NSC, L);
}

__global__ void __launch_bounds__(256) k_km(unsigned char* __restrict__ ws) {
  const int lane = threadIdx.x & 63, g = lane >> 4, n = lane & 15;
  const int wv = blockIdx.x * 4 + (threadIdx.x >> 6);
  const int sg = wv & 15, wj = wv >> 4, h = wj >> 1, half = wj & 1;
  const unsigned char* wsrc = ws + OFF_WF + ((size_t)(h * 24 + 12 + half * 6) * 20) * 1024 + lane * 16;
  const unsigned char* msrc = ws + OFF_MEM + (size_t)(sg * 4) * 10240 + (g * 8 + (n & 7)) * 16;
  f32x4 acc[6][4];
#pragma unroll
  for (int i = 0; i < 6; ++i)
#pragma unroll
    for (int m = 0; m < 4; ++m) acc[i][m] = f32x4{0, 0, 0, 0};
#pragma unroll 2
  for (int kk = 0; kk < KS; ++kk) {
    v4u b[4];
#pragma unroll
    for (int m = 0; m < 4; ++m) b[m] = *(const v4u*)(msrc + m * 10240 + kk * 512);
#pragma unroll
    for (int i = 0; i < 6; ++i) {
      v4u a = *(const v4u*)(wsrc + (i * 20 + kk) * 1024);
#pragma unroll
      for (int m = 0; m < 4; ++m) acc[i][m] = mfma32(a, b[m], acc[i][m]);
    }
  }
#pragma unroll
  for (int m = 0; m < 4; ++m) {
    unsigned char* dst = ws + OFF_KM + ((size_t)((sg * 4 + m) * 32 + h) * 6) * 1024 + lane * 16;
#pragma unroll
    for (int p = 0; p < 3; ++p) {
      f32x4 lo = acc[2 * p][m], hi = acc[2 * p + 1][m];
      v4u o = {pack2(lo[0], lo[1]), pack2(lo[2], lo[3]), pack2(hi[0], hi[1]), pack2(hi[2], hi[3])};
      *(v4u*)(dst + (half * 3 + p) * 1024) = o;
    }
  }
}

__global__ void __launch_bounds__(256) k_px(unsigned char* __restrict__ ws) {
  const int lane = threadIdx.x & 63, g = lane >> 4, n = lane & 15;
  const int wv = blockIdx.x * 4 + (threadIdx.x >> 6);
  const int rt = wv & 1, h = (wv >> 1) & 31, s = wv >> 6;
  const unsigned char* q = ws + OFF_QX + ((size_t)(s * 32 + h) * 12 + rt * 6) * 1024 + lane * 16;
  const unsigned char* kx = ws + OFF_KX + ((size_t)(s * 32 + h) * 12) * 1024 + lane * 16;
  const unsigned char* km = ws + OFF_KM + ((size_t)(s * 32 + h) * 6) * 1024 + lane * 16;
  f32x4 sm = {0, 0, 0, 0}, s0 = {0, 0, 0, 0}, s1 = {0, 0, 0, 0};
#pragma unroll
  for (int p = 0; p < 6; ++p) {
    const v4u qq = *(const v4u*)(q + p * 1024);
    sm = mfma32(*(const v4u*)(km + p * 1024), qq, sm);
    s0 = mfma32(*(const v4u*)(kx + p * 1024), qq, s0);
    s1 = mfma32(*(const v4u*)(kx + (6 + p) * 1024), qq, s1);
  }
  const float NEG = -3.0e38f;
  const int t = rt * 16 + n;
  if (g >= 2) sm = f32x4{NEG, NEG, NEG, NEG};
#pragma unroll
  for (int r = 0; r < 4; ++r) {
    if (4 * g + r > t) s0[r] = NEG;
    if (16 + 4 * g + r > t) s1[r] = NEG;
  }
  float mx = fmaxf(fmaxf(fmaxf(sm[0], sm[1]), fmaxf(sm[2], sm[3])), fmaxf(fmaxf(fmaxf(s0[0], s0[1]), fmaxf(s0[2], s0[3])), fmaxf(fmaxf(s1[0], s1[1]), fmaxf(s1[2], s1[3]))));
  mx = xmax4(mx);
  float em[4], e0[4], e1[4], sum = 0.f;
#pragma unroll
  for (int r = 0; r < 4; ++r) {
    em[r] = __builtin_amdgcn_exp2f(sm[r] - mx); e0[r] = __builtin_amdgcn_exp2f(s0[r] - mx); e1[r] = __builtin_amdgcn_exp2f(s1[r] - mx);
    sum += em[r] + e0[r] + e1[r];
  }
  sum = xsum4(sum);
  const float inv = 1.f / sum;
  v4u px = {pack2(e0[0] * inv, e0[1] * inv), pack2(e0[2] * inv, e0[3] * inv), pack2(e1[0] * inv, e1[1] * inv), pack2(e1[2] * inv, e1[3] * inv)};
  v2u pm = {pack2(em[0] * inv, em[1] * inv), pack2(em[2] * inv, em[3] * inv)};
  const int rg = 2 * rt + (n >> 3), l2 = g * 16 + (h & 1) * 8 + (n & 7);
  unsigned char* dst = ws + OFF_PX + ((size_t)(s * 16 + (h >> 1)) * 4 + rg) * 1536;
  *(v4u*)(dst + l2 * 16) = px;
  *(v2u*)(dst + 1024 + l2 * 8) = pm;
}

__global__ void __launch_bounds__(640) k_attnout(const u16* __restrict__ x, unsigned char* __restrict__ ws) {
  extern __shared__ __attribute__((aligned(16))) unsigned char smem[];
  LAS unsigned char* L = (LAS unsigned char*)smem;
  const int tid = threadIdx.x, lane = tid & 63, g = lane >> 4, n = lane & 15;
  const int w = __builtin_amdgcn_readfirstlane(tid >> 6);
  const int s = blockIdx.x >> 2, qt = blockIdx.x & 3;
  {
    const unsigned char* src = ws + OFF_PX + (size_t)s * 98304;
    for (int i = w; i < 96; i += 10)
      __builtin_amdgcn_global_load_lds((const GAS void*)(src + i * 1024 + lane * 16), (LAS void*)(L + i * 1024), 16, 0, 0);
  }
  __syncthreads();
  const int c0 = qt * 20 + 2 * w;
  f32x4 acc[2][4];
#pragma unroll
  for (int c = 0; c < 2; ++c)
#pragma unroll
    for (int r = 0; r < 4; ++r) acc[c][r] = f32x4{0, 0, 0, 0};
  const unsigned char* vsrc = ws + OFF_VO + ((size_t)(s * 80 + c0) * 16) * 1280;
#pragma unroll 2
  for (int hp = 0; hp < 16; ++hp) {
    v4u vx[2]; v2u vm[2];
#pragma unroll
    for (int c = 0; c < 2; ++c) {
      vx[c] = *(const v4u*)(vsrc + (size_t)(c * 16 + hp) * 1280 + lane * 16);
      vm[c] = *(const v2u*)(vsrc + (size_t)(c * 16 + hp) * 1280 + 1024 + (lane & 31) * 8);
    }
#pragma unroll
    for (int rg = 0; rg < 4; ++rg) {
      const v4u px = *(LAS v4u*)(L + (hp * 4 + rg) * 1536 + lane * 16);
      const v2u pm = *(LAS v2u*)(L + (hp * 4 + rg) * 1536 + 1024 + lane * 8);
#pragma unroll
      for (int c = 0; c < 2; ++c) {
        acc[c][rg] = mfma32(vx[c], px, acc[c][rg]);
        acc[c][rg] = mfma16(vm[c], pm, acc[c][rg]);
      }
    }
  }
  float* hout = (float*)(ws + OFF_H);
#pragma unroll
  for (int c = 0; c < 2; ++c)
#pragma unroll
    for (int rg = 0; rg < 4; ++rg) {
      f32x4 o;
#pragma unroll
      for (int r = 0; r < 4; ++r) o[r] = acc[c][rg][r] + __shfl(acc[c][rg][r], (lane + 40) & 63);
      if (g < 2 && n < 8) {
        const int t = s * 32 + rg * 8 + n, col = (c0 + c) * 8 + g * 4;
        const v2u xv = *(const v2u*)(x + (size_t)t * DM + col);
        o[0] += bflo(xv.x); o[1] += bfhi(xv.x); o[2] += bflo(xv.y); o[3] += bfhi(xv.y);
        *(f32x4*)(hout + (size_t)t * DM + col) = o;
      }
    }
}

__global__ void __launch_bounds__(256) k_ln2(unsigned char* __restrict__ ws) {
  const int t = threadIdx.x, lane = t & 63, w = t >> 6;
  const int row = blockIdx.x * 16 + w * 4 + (lane >> 4), q = lane & 15;
  const float* hsrc = (const float*)(ws + OFF_H) + (size_t)row * DM;
  float v[5][8];
  float s = 0.f;
#pragma unroll
  for (int i = 0; i < 5; ++i) {
    f32x4 a = *(const f32x4*)(hsrc + (q + 16 * i) * 8), b = *(const f32x4*)(hsrc + (q + 16 * i) * 8 + 4);
#pragma unroll
    for (int j = 0; j < 4; ++j) { v[i][j] = a[j]; v[i][4 + j] = b[j]; s += a[j] + b[j]; }
  }
#pragma unroll
  for (int o = 1; o < 16; o <<= 1) s += __shfl_xor(s, o);
  const float mean = s * (1.f / DM);
  float qq = 0.f;
#pragma unroll
  for (int i = 0; i < 5; ++i)
#pragma unroll
    for (int j = 0; j < 8; ++j) { float d = v[i][j] - mean; qq += d * d; }
#pragma unroll
  for (int o = 1; o < 16; o <<= 1) qq += __shfl_xor(qq, o);
  const float rs = 1.f / sqrtf(qq * (1.f / DM) + 1e-5f);
  unsigned char* outF = ws + OFF_HNF;
#pragma unroll
  for (int i = 0; i < 5; ++i) {
    const int pc = q + 16 * i, kk = pc >> 2, g = pc & 3;
    v4u o;
    o.x = pack2((v[i][0] - mean) * rs, (v[i][1] - mean) * rs);
    o.y = pack2((v[i][2] - mean) * rs, (v[i][3] - mean) * rs);
    o.z = pack2((v[i][4] - mean) * rs, (v[i][5] - mean) * rs);
    o.w = pack2((v[i][6] - mean) * rs, (v[i][7] - mean) * rs);
    *(v4u*)(outF + ((size_t)(row >> 4) * 20 + kk) * 1024 + (g * 16 + (row & 15)) * 16) = o;
  }
}

__device__ __forceinline__ float gelu_tanh(float x) {
  const float u = 0.7978845608028654f * (x + 0.044715f * x * x * x);
  const float e = __expf(2.f * u);
  const float th = 1.f - 2.f / (e + 1.f);
  return 0.5f * x * (1.f + th);
}
__global__ void __launch_bounds__(256) k_ffn1(unsigned char* __restrict__ ws) {
  const int lane = threadIdx.x & 63;
  const int bw = blockIdx.x;
  const int rgp = bw & 15, fp = bw >> 4;
  const int rt0 = (rgp * 4 + (threadIdx.x >> 6)) * 2;
  const unsigned char* wsrc = ws + OFF_WGU + ((size_t)(fp * 2) * 2 * 20) * 1024 + lane * 16;
  const unsigned char* xsrc = ws + OFF_HNF + ((size_t)rt0 * 20) * 1024 + lane * 16;
  f32x4 acc[4][2];
#pragma unroll
  for (int i = 0; i < 4; ++i) { acc[i][0] = f32x4{0, 0, 0, 0}; acc[i][1] = f32x4{0, 0, 0, 0}; }
#pragma unroll 2
  for (int kk = 0; kk < KS; ++kk) {
    v4u b0 = *(const v4u*)(xsrc + kk * 1024), b1 = *(const v4u*)(xsrc + (20 + kk) * 1024);
#pragma unroll
    for (int i = 0; i < 4; ++i) {
      v4u a = *(const v4u*)(wsrc + (i * 20 + kk) * 1024);
      acc[i][0] = mfma32(a, b0, acc[i][0]);
      acc[i][1] = mfma32(a, b1, acc[i][1]);
    }
  }
#pragma unroll
  for (int m = 0; m < 2; ++m) {
    float o[8];
#pragma unroll
    for (int r = 0; r < 4; ++r) {
      o[r] = gelu_tanh(acc[0][m][r]) * acc[1][m][r];
      o[4 + r] = gelu_tanh(acc[2][m][r]) * acc[3][m][r];
    }
    v4u ov = {pack2(o[0], o[1]), pack2(o[2], o[3]), pack2(o[4], o[5]), pack2(o[6], o[7])};
    *(v4u*)(ws + OFF_AF + ((size_t)(rt0 + m) * 48 + fp) * 1024 + lane * 16) = ov;
  }
}

__global__ void __launch_bounds__(256) k_ffn2(unsigned char* __restrict__ ws, u16* __restrict__ out) {
  const int lane = threadIdx.x & 63, g = lane >> 4, n = lane & 15;
  const int bw = blockIdx.x;
  const int rgp = bw & 15, cg = bw >> 4;
  const int rt0 = (rgp * 4 + (threadIdx.x >> 6)) * 2;
  const unsigned char* wsrc = ws + OFF_WD + ((size_t)(cg * 4) * 48) * 1024 + lane * 16;
  const unsigned char* xsrc = ws + OFF_AF + ((size_t)rt0 * 48) * 1024 + lane * 16;
  f32x4 acc[4][2];
#pragma unroll
  for (int i = 0; i < 4; ++i) { acc[i][0] = f32x4{0, 0, 0, 0}; acc[i][1] = f32x4{0, 0, 0, 0}; }
#pragma unroll 2
  for (int kk = 0; kk < 48; ++kk) {
    v4u b0 = *(const v4u*)(xsrc + kk * 1024), b1 = *(const v4u*)(xsrc + (48 + kk) * 1024);
#pragma unroll
    for (int i = 0; i < 4; ++i) {
      v4u a = *(const v4u*)(wsrc + (i * 48 + kk) * 1024);
      acc[i][0] = mfma32(a, b0, acc[i][0]);
      acc[i][1] = mfma32(a, b1, acc[i][1]);
    }
  }
  const float* hsrc = (const float*)(ws + OFF_H);
#pragma unroll
  for (int m = 0; m < 2; ++m)
#pragma unroll
    for (int i = 0; i < 4; ++i) {
      const int t = (rt0 + m) * 16 + n, col = (cg * 4 + i) * 16 + g * 4;
      const f32x4 hv = *(const f32x4*)(hsrc + (size_t)t * DM + col);
      v2u o = {pack2(acc[i][m][0] + hv[0], acc[i][m][1] + hv[1]), pack2(acc[i][m][2] + hv[2], acc[i][m][3] + hv[3])};
      *(v2u*)(out + (size_t)t * DM + col) = o;
    }
}

static void run(void* const* d_in, void* d_out, void* d_ws, hipStream_t st) {
  const u16* x = (const u16*)d_in[0]; const u16* mem0 = (const u16*)d_in[1];
  const u16* Wq = (const u16*)d_in[2]; const u16* Wk = (const u16*)d_in[3]; const u16* Wv = (const u16*)d_in[4]; const u16* Wo = (const u16*)d_in[5];
  const u16* wg = (const u16*)d_in[8]; const u16* wu = (const u16*)d_in[9]; const u16* wd = (const u16*)d_in[10];
  unsigned char* ws = (unsigned char*)d_ws;
  (void)hipFuncSetAttribute((const void*)k_chain, hipFuncAttributeMaxDynamicSharedMemorySize, CH_LDS);
  (void)hipFuncSetAttribute((const void*)k_attnout, hipFuncAttributeMaxDynamicSharedMemorySize, 98304);
  (void)hipMemsetAsync(ws, 0xFF, SZ_SENT, st);
  k_pre<<<PT_BLOCKS + LN_BLOCKS + 1, 256, 0, st>>>(x, mem0, Wq, Wk, Wo, wg, wu, wd, ws);
  k_wvo<<<320, 256, 0, st>>>(Wv, ws);
  k_qkx<<<16 * 128, 256, 0, st>>>(ws);
  k_chain<<<NSC + NCOL, CH_T, CH_LDS, st>>>(ws);
  k_km<<<256, 256, 0, st>>>(ws);
  k_px<<<1024, 256, 0, st>>>(ws);
  k_attnout<<<256, 640, 98304, st>>>(x, ws);
  k_ln2<<<128, 256, 0, st>>>(ws);
  k_ffn1<<<16 * 48, 256, 0, st>>>(ws);
  k_ffn2<<<160, 256, 0, st>>>(ws, (u16*)d_out);
}
}

extern "C" void kernel_launch(void* const* d_in, const int* in_sizes, int n_in, void* d_out, int out_size, void* d_ws, size_t ws_size,
                              hipStream_t stream) {
  rmt::run(d_in, d_out, d_ws, stream);
}
```

```cpp
#include <hip/hip_runtime.h>
#include <hip/hip_bf16.h>
#include <stdint.h>

namespace rmt {
typedef unsigned short u16;
typedef unsigned v4u __attribute__((ext_vector_type(4)));
typedef unsigned v2u __attribute__((ext_vector_type(2)));
typedef float f32x4 __attribute__((ext_vector_type(4)));
typedef short bf16x8 __attribute__((ext_vector_type(8)));
typedef short bf16x4 __attribute__((ext_vector_type(4)));
typedef __bf16 bfv2 __attribute__((ext_vector_type(2)));
#define LAS __attribute__((address_space(3)))
#define GAS __attribute__((address_space(1)))

constexpr int S = 2048, DM = 640, H = 32, DH = 192, HD = H * DH, SEG = 32, NSEG = 64, DF = 1536;
constexpr int KS = DM / 32;
constexpr unsigned SENT = 0xFFFFFFFFu;
constexpr int AUX_SC1 = 16;

constexpr int XB_SLOTS = 64 * 65536;
constexpr int XB_CTL = XB_SLOTS;
constexpr int XB_POL = XB_CTL + 128, XB_PROG = XB_CTL + 256, XB_CNTA = XB_CTL + 384, XB_CNTB = XB_CTL + 512, XB_CNTALL = XB_CTL + 640,
              XB_CNTF = XB_CTL + 768;
constexpr int XB_XFLAG = XB_CTL + 1024;
constexpr int XB_KMFLAG = XB_XFLAG + 64 * 32 * 64;
constexpr int XB_SIZE = XB_KMFLAG + 64 * 32 * 64;
constexpr size_t OFF_XNF = (size_t)8 << 20;
constexpr size_t OFF_WF = OFF_XNF + (size_t)128 * 20 * 1024;
constexpr size_t OFF_WVF = OFF_WF + (size_t)32 * 24 * 20 * 1024;
constexpr size_t OFF_WGU = OFF_WVF + (size_t)80 * 16 * 20 * 1024;
constexpr size_t OFF_WD = OFF_WGU + (size_t)96 * 2 * 20 * 1024;
constexpr size_t OFF_QX = OFF_WD + (size_t)40 * 48 * 1024;
constexpr size_t OFF_KX = OFF_QX + (size_t)64 * 32 * 12 * 1024;
constexpr size_t OFF_KM = OFF_KX + (size_t)64 * 32 * 12 * 1024;
constexpr size_t OFF_PX = OFF_KM + (size_t)64 * 32 * 6 * 1024;
constexpr size_t OFF_VO = OFF_PX + (size_t)64 * 16 * 4 * 1536;
constexpr size_t OFF_H = OFF_VO + (size_t)64 * 80 * 16 * 1280;
constexpr size_t OFF_WOF = OFF_H;
constexpr size_t OFF_HNF = OFF_H + (size_t)S * DM * 4;
constexpr size_t OFF_AF = OFF_HNF + (size_t)128 * 20 * 1024;
constexpr size_t WS_END = OFF_AF + (size_t)128 * 48 * 1024;
static_assert(OFF_WOF + (size_t)40 * 192 * 1024 <= WS_END, "WOF alias");
static_assert((size_t)XB_SIZE + (2u << 20) <= OFF_XNF, "xb");
static_assert(WS_END <= (size_t)256 * 1024 * 1024, "workspace");

__device__ __forceinline__ float bf2f(unsigned v) { return __uint_as_float(v << 16); }
__device__ __forceinline__ float bflo(unsigned v) { return __uint_as_float(v << 16); }
__device__ __forceinline__ float bfhi(unsigned v) { return __uint_as_float(v & 0xFFFF0000u); }
__device__ __forceinline__ unsigned pack2(float a, float b) {
  bfv2 v = {(__bf16)a, (__bf16)b};
  return __builtin_bit_cast(unsigned, v);
}
__device__ __forceinline__ unsigned nosent(unsigned d) { return d == SENT ? 0x7FC07FC0u : d; }
__device__ __forceinline__ f32x4 mfma32(v4u a, v4u b, f32x4 c) {
  return __builtin_amdgcn_mfma_f32_16x16x32_bf16(__builtin_bit_cast(bf16x8, a), __builtin_bit_cast(bf16x8, b), c, 0, 0, 0);
}
__device__ __forceinline__ f32x4 mfma16(v2u a, v2u b, f32x4 c) {
  return __builtin_amdgcn_mfma_f32_16x16x16bf16_1k(__builtin_bit_cast(bf16x4, a), __builtin_bit_cast(bf16x4, b), c, 0, 0, 0);
}
__device__ __forceinline__ float xmax4(float x) {
  auto s = __builtin_amdgcn_permlane16_swap(__float_as_uint(x), __float_as_uint(x), false, false);
  x = fmaxf(__uint_as_float(s[0]), __uint_as_float(s[1]));
  auto t = __builtin_amdgcn_permlane32_swap(__float_as_uint(x), __float_as_uint(x), false, false);
  return fmaxf(__uint_as_float(t[0]), __uint_as_float(t[1]));
}
__device__ __forceinline__ float xsum4(float x) {
  auto s = __builtin_amdgcn_permlane16_swap(__float_as_uint(x), __float_as_uint(x), false, false);
  x = __uint_as_float(s[0]) + __uint_as_float(s[1]);
  auto t = __builtin_amdgcn_permlane32_swap(__float_as_uint(x), __float_as_uint(x), false, false);
  return __uint_as_float(t[0]) + __uint_as_float(t[1]);
}
__device__ __forceinline__ int xmap(int o) { return (o >> 13) * 16384 + (o & 8191); }
__device__ __forceinline__ bool valid4(v4u v) { return (v.x != SENT) & (v.y != SENT) & (v.z != SENT) & (v.w != SENT); }
__device__ __forceinline__ bool valid2(v2u v) { return (v.x != SENT) & (v.y != SENT); }

constexpr int NB_WQ = 20 * 48, NB_WO = 192 * 5, NB_WG = 20 * 12, NB_WD = 48 * 5;
constexpr int PT_BLOCKS = 2 * NB_WQ + NB_WO + 2 * NB_WG + NB_WD;
constexpr int LN_BLOCKS = S / 16;

__device__ __forceinline__ void ln_rows_bf16(const u16* __restrict__ x, unsigned char* __restrict__ outF, int blk) {
  const int t = threadIdx.x, lane = t & 63, w = t >> 6;
  const int row = blk * 16 + w * 4 + (lane >> 4), q = lane & 15;
  float v[5][8];
  float s = 0.f;
#pragma unroll
  for (int i = 0; i < 5; ++i) {
    v4u d = *(const v4u*)(x + (size_t)row * DM + (q + 16 * i) * 8);
    v[i][0] = bflo(d.x); v[i][1] = bfhi(d.x); v[i][2] = bflo(d.y); v[i][3] = bfhi(d.y);
    v[i][4] = bflo(d.z); v[i][5] = bfhi(d.z); v[i][6] = bflo(d.w); v[i][7] = bfhi(d.w);
#pragma unroll
    for (int j = 0; j < 8; ++j) s += v[i][j];
  }
#pragma unroll
  for (int o = 1; o < 16; o <<= 1) s += __shfl_xor(s, o);
  const float mean = s * (1.f / DM);
  float qq = 0.f;
#pragma unroll
  for (int i = 0; i < 5; ++i)
#pragma unroll
    for (int j = 0; j < 8; ++j) { float d = v[i][j] - mean; qq += d * d; }
#pragma unroll
  for (int o = 1; o < 16; o <<= 1) qq += __shfl_xor(qq, o);
  const float rs = 1.f / sqrtf(qq * (1.f / DM) + 1e-5f);
#pragma unroll
  for (int i = 0; i < 5; ++i) {
    const int pc = q + 16 * i, kk = pc >> 2, g = pc & 3;
    v4u o;
    o.x = pack2((v[i][0] - mean) * rs, (v[i][1] - mean) * rs);
    o.y = pack2((v[i][2] - mean) * rs, (v[i][3] - mean) * rs);
    o.z = pack2((v[i][4] - mean) * rs, (v[i][5] - mean) * rs);
    o.w = pack2((v[i][6] - mean) * rs, (v[i][7] - mean) * rs);
    *(v4u*)(outF + ((size_t)(row >> 4) * 20 + kk) * 1024 + (g * 16 + (row & 15)) * 16) = o;
  }
}

__global__ void __launch_bounds__(256) k_pre(const u16* __restrict__ x, const u16* __restrict__ mem0, const u16* __restrict__ Wq,
                                             const u16* __restrict__ Wk, const u16* __restrict__ Wo, const u16* __restrict__ wg,
                                             const u16* __restrict__ wu, const u16* __restrict__ wd, unsigned char* __restrict__ ws,
                                             unsigned char* __restrict__ xb) {
  __shared__ unsigned tile[32 * 65];
  int b = blockIdx.x;
  const int t = threadIdx.x;
  if (b >= PT_BLOCKS) {
    b -= PT_BLOCKS;
    if (b < LN_BLOCKS) { ln_rows_bf16(x, ws + OFF_XNF, b); return; }
    for (int pc = t; pc < 640; pc += 256) {
      const int c8 = pc >> 3, i = pc & 7;
      v4u d = *(const v4u*)(mem0 + i * DM + c8 * 8);
      d.x = nosent(d.x); d.y = nosent(d.y); d.z = nosent(d.z); d.w = nosent(d.w);
      *(v4u*)(xb + xmap(pc * 16)) = d;
      *(v4u*)(xb + xmap(pc * 16) + 8192) = d;
    }
    return;
  }
  const u16* src; int ld, kk, nb, job;
  if (b < NB_WQ) { job = 0; src = Wq; ld = HD; kk = b / 48; nb = b % 48; }
  else if (b < 2 * NB_WQ) { b -= NB_WQ; job = 1; src = Wk; ld = HD; kk = b / 48; nb = b % 48; }
  else if (b < 2 * NB_WQ + NB_WO) { b -= 2 * NB_WQ; job = 2; src = Wo; ld = DM; kk = b / 5; nb = b % 5; }
  else if (b < 2 * NB_WQ + NB_WO + NB_WG) { b -= 2 * NB_WQ + NB_WO; job = 3; src = wg; ld = DF; kk = b / 12; nb = b % 12; }
  else if (b < 2 * NB_WQ + NB_WO + 2 * NB_WG) { b -= 2 * NB_WQ + NB_WO + NB_WG; job = 4; src = wu; ld = DF; kk = b / 12; nb = b % 12; }
  else { b -= 2 * NB_WQ + NB_WO + 2 * NB_WG; job = 5; src = wd; ld = DM; kk = b / 5; nb = b % 5; }
  {
    const int row = t >> 3, ch = t & 7;
    const v4u* p = (const v4u*)(src + (size_t)(kk * 32 + row) * ld + nb * 128 + ch * 16);
    v4u a = p[0], c = p[1];
    unsigned* d = tile + row * 65 + ch * 8;
    d[0] = a.x; d[1] = a.y; d[2] = a.z; d[3] = a.w; d[4] = c.x; d[5] = c.y; d[6] = c.z; d[7] = c.w;
  }
  __syncthreads();
  const u16* tl = (const u16*)tile;
#pragma unroll
  for (int it = 0; it < 2; ++it) {
    const int pc = t + it * 256, i = pc >> 6, lane = pc & 63, g = lane >> 4, n = lane & 15;
    unsigned e[8];
#pragma unroll
    for (int j = 0; j < 8; ++j) {
      const int row = (job == 5) ? (16 * (j >> 2) + 4 * g + (j & 3)) : (8 * g + j);
      e[j] = tl[row * 130 + i * 16 + n];
    }
    v4u o = {e[0] | (e[1] << 16), e[2] | (e[3] << 16), e[4] | (e[5] << 16), e[6] | (e[7] << 16)};
    const int nt = nb * 8 + i;
    size_t dst;
    if (job <= 1) { const int h = nt / 12, et = nt % 12; dst = OFF_WF + ((size_t)(h * 24 + job * 12 + et) * 20 + kk) * 1024; }
    else if (job == 2) dst = OFF_WOF + ((size_t)nt * 192 + kk) * 1024;
    else if (job <= 4) dst = OFF_WGU + ((size_t)(nt * 2 + (job - 3)) * 20 + kk) * 1024;
    else dst = OFF_WD + ((size_t)nt * 48 + kk) * 1024;
    *(v4u*)(ws + dst + lane * 16) = o;
  }
}

__global__ void __launch_bounds__(256) k_wvo(const u16* __restrict__ Wv, unsigned char* __restrict__ ws) {
  const int lane = threadIdx.x & 63, g = lane >> 4, n = lane & 15;
  const int wv = blockIdx.x * 4 + (threadIdx.x >> 6);
  const int h = wv / 40, rem = wv % 40, mg = rem >> 2, cq = rem & 3;
  v4u a[4][6];
#pragma unroll
  for (int m = 0; m < 4; ++m)
#pragma unroll
    for (int ke = 0; ke < 6; ++ke)
      a[m][ke] = *(const v4u*)(Wv + (size_t)(32 * (2 * mg + (m >> 1)) + 8 * (n >> 2) + 4 * (m & 1) + (n & 3)) * HD + h * DH + ke * 32 + g * 8);
  for (int ci = 0; ci < 10; ++ci) {
    const int ct = cq * 10 + ci;
    v4u bf[6];
#pragma unroll
    for (int ke = 0; ke < 6; ++ke) bf[ke] = *(const v4u*)(ws + OFF_WOF + ((size_t)ct * 192 + h * 6 + ke) * 1024 + lane * 16);
    f32x4 acc[4];
#pragma unroll
    for (int m = 0; m < 4; ++m) {
      acc[m] = f32x4{0.f, 0.f, 0.f, 0.f};
#pragma unroll
      for (int ke = 0; ke < 6; ++ke) acc[m] = mfma32(a[m][ke], bf[ke], acc[m]);
    }
    const int c8 = 2 * ct + (n >> 3), hp = h >> 1, n2 = (h & 1) * 8 + (n & 7);
#pragma unroll
    for (int kq = 0; kq < 2; ++kq) {
      const f32x4 lo = acc[2 * kq], hi = acc[2 * kq + 1];
      const v4u o = {pack2(lo[0], lo[1]), pack2(lo[2], lo[3]), pack2(hi[0], hi[1]), pack2(hi[2], hi[3])};
      *(v4u*)(ws + OFF_WVF + (((size_t)(c8 * 16 + hp) * 20 + 2 * mg + kq) * 64 + g * 16 + n2) * 16) = o;
    }
  }
}

constexpr int NSC = 32, NCOL = 80, NCH = NSC + NCOL, NXS = 64, CH_T = 512, CH_GRID = 256;
constexpr unsigned SPIN_CAP = 1u << 22;
constexpr float QSCALE = 0.07216878364870322f * 1.4426950408889634f;
constexpr int SL_W = 0, SL_MEM = 96 * 1024, SL_QK = SL_MEM + 10240, SL_KX = SL_QK + 12 * 1024, SL_ROLE = SL_KX + 24 * 1024, SL_END = SL_ROLE + 64;
constexpr int CL_X = 0, CL_MEM = 80 * 1024, CL_PART = CL_MEM + 10240, CL_END = CL_PART + 4096;
constexpr int XL_X = 0, XL_MEM = 80 * 1024, XL_CNT = XL_MEM + 10240, XL_END = XL_CNT + 64;
constexpr int CH_LDS = SL_END;
static_assert(CH_LDS <= 160 * 1024 && CL_END <= SL_ROLE && XL_END <= SL_ROLE, "lds");

#define STAMP(blk, s, k) do {} while (0)
#define SCHED_FENCE() __builtin_amdgcn_sched_barrier(0)
#define RAW_BARRIER() do { asm volatile("s_waitcnt lgkmcnt(0)" ::: "memory"); __builtin_amdgcn_s_barrier(); asm volatile("" ::: "memory"); } while (0)
__device__ __forceinline__ v4u ldsr4(LAS unsigned char* p) { return *(LAS v4u*)p; }
__device__ __forceinline__ float max3f(float a, float b, float c) { float r; asm("v_max3_f32 %0, %1, %2, %3" : "=v"(r) : "v"(a), "v"(b), "v"(c)); return r; }
__device__ __forceinline__ float max2f(float a, float b) { float r; asm("v_max_f32 %0, %1, %2" : "=v"(r) : "v"(a), "v"(b)); return r; }
__device__ __forceinline__ float ror8(float x) {
  return __builtin_bit_cast(float, __builtin_amdgcn_update_dpp(0, __builtin_bit_cast(int, x), 0x128, 0xf, 0xf, false));
}
__device__ __forceinline__ float xmax8(float x) {
  x = max2f(x, ror8(x));
  auto s = __builtin_amdgcn_permlane16_swap(__float_as_uint(x), __float_as_uint(x), false, false);
  x = max2f(__uint_as_float(s[0]), __uint_as_float(s[1]));
  auto t = __builtin_amdgcn_permlane32_swap(__float_as_uint(x), __float_as_uint(x), false, false);
  return max2f(__uint_as_float(t[0]), __uint_as_float(t[1]));
}
__device__ __forceinline__ float xsum8(float x) { x += ror8(x); return xsum4(x); }
__device__ __forceinline__ void give_up(__amdgpu_buffer_rsrc_t rs) { __builtin_amdgcn_raw_buffer_store_b32(1u, rs, XB_CTL, 0, AUX_SC1); }
constexpr int POL_FROM = 4;
__device__ __forceinline__ int slot_off(int s, unsigned pol) { return s * 65536 + (s >= POL_FROM ? (int)((pol >> (s >> 5)) & 1u) * 8192 : 0); }

__device__ __forceinline__ bool gather_mem(__amdgpu_buffer_rsrc_t rs, int soff, int w, int lane, LAS unsigned char* img) {
  const int p0 = 80 * w + lane, p1 = 80 * w + 64 + (lane & 15);
  const int o0 = xmap(p0 * 16), o1 = xmap(p1 * 16);
  v4u v0, v1;
  for (unsigned spins = 0;; ++spins) {
    asm volatile("" ::: "memory");
    v0 = __builtin_amdgcn_raw_buffer_load_b128(rs, o0, soff, AUX_SC1);
    v1 = __builtin_amdgcn_raw_buffer_load_b128(rs, o1, soff, AUX_SC1);
    if (__all(valid4(v0) & valid4(v1))) break;
    if (spins > SPIN_CAP) { give_up(rs); return false; }
  }
  *(LAS v4u*)(img + p0 * 16) = v0;
  if (lane < 16) *(LAS v4u*)(img + p1 * 16) = v1;
  return true;
}
__device__ __forceinline__ bool wait_flags2(__amdgpu_buffer_rsrc_t rs, int off) {
  for (unsigned spins = 0;; ++spins) {
    asm volatile("" ::: "memory");
    v2u f = __builtin_amdgcn_raw_buffer_load_b64(rs, off, 0, AUX_SC1);
    if (__all(valid2(f))) break;
    if (spins > SPIN_CAP) { give_up(rs); return false; }
    __builtin_amdgcn_s_sleep(8);
  }
  asm volatile("" ::: "memory");
  return true;
}
__device__ __forceinline__ unsigned wait_pol(__amdgpu_buffer_rsrc_t rs) {
  unsigned v;
  for (unsigned spins = 0;; ++spins) {
    asm volatile("" ::: "memory");
    v = __builtin_amdgcn_raw_buffer_load_b32(rs, XB_POL, 0, AUX_SC1);
    if (v != SENT) break;
    if (spins > SPIN_CAP) { give_up(rs); v = 0; break; }
    __builtin_amdgcn_s_sleep(2);
  }
  return (unsigned)__builtin_amdgcn_readfirstlane((int)v);
}

__device__ __forceinline__ void score_role(unsigned char* __restrict__ ws, __amdgpu_buffer_rsrc_t rs, int h, LAS unsigned char* L) {
  const int tid = threadIdx.x, lane = tid & 63, g = lane >> 4, n = lane & 15;
  const int w = __builtin_amdgcn_readfirstlane(tid >> 6);
  const __amdgpu_buffer_rsrc_t rkx = __builtin_amdgcn_make_buffer_rsrc((void*)(ws + OFF_KX), 0, 64 * 32 * 12 * 1024, 0x00020000);
  v4u wr[48];
  {
    const unsigned char* src = ws + OFF_WF + ((size_t)(h * 24 + 3 * w) * 20) * 1024 + lane * 16;
#pragma unroll
    for (int i = 0; i < 48; ++i) wr[i] = *(const v4u*)(src + i * 1024);
#pragma unroll
    for (int i = 48; i < 60; ++i) *(LAS v4u*)(L + SL_W + (w * 12 + i - 48) * 1024 + lane * 16) = *(const v4u*)(src + i * 1024);
  }
  auto fetch_kx = [&](int seg) -> bool {
    if (!wait_flags2(rs, XB_XFLAG + (seg * 32 + h) * 64)) return false;
    const int src = ((seg * 32 + h) * 12) * 1024 + lane * 16;
    LAS unsigned char* dst = L + SL_KX + (seg & 1) * 12288 + lane * 16;
    const v4u a = __builtin_amdgcn_raw_buffer_load_b128(rkx, src + w * 1024, 0, AUX_SC1);
    const v4u b = __builtin_amdgcn_raw_buffer_load_b128(rkx, src + (8 + (w & 3)) * 1024, 0, AUX_SC1);
    *(LAS v4u*)(dst + w * 1024) = a;
    if (w < 4) *(LAS v4u*)(dst + (8 + w) * 1024) = b;
    return true;
  };
  unsigned pol = 0;
  if (!fetch_kx(0)) return;
  const float qs = w < 4 ? QSCALE : 1.f;
  LAS unsigned char* memrd = L + SL_MEM + (g * 8 + (n & 7)) * 16;
  LAS unsigned char* wl = L + SL_W + (w * 12) * 1024 + lane * 16;
  const int pox = xmap(10240 + h * 640 + (g * 8 + (n & 7)) * 16 + (n >> 3) * 8);
  const int pom = xmap(10240 + h * 640 + 512 + ((g & 1) * 8 + (n & 7)) * 8 + (n >> 3) * 4);
  for (int s = 0; s < NSEG - 1; ++s) {
    if (s == POL_FROM) pol = wait_pol(rs);
    const int soff = slot_off(s, pol);
    STAMP(h, s, 0);
    if (!gather_mem(rs, soff, w, lane, L + SL_MEM)) return;
    STAMP(h, s, 1);
    RAW_BARRIER();
    STAMP(h, s, 2);
    f32x4 acc[3] = {{0, 0, 0, 0}, {0, 0, 0, 0}, {0, 0, 0, 0}};
    {
      v4u bq[3], aq[2];
      bq[0] = ldsr4(memrd); bq[1] = ldsr4(memrd + 512); bq[2] = ldsr4(memrd + 1024);
      SCHED_FENCE();
#pragma unroll
      for (int kk = 0; kk < KS; ++kk) {
        const v4u b = bq[kk % 3];
        v4u a2;
        if (kk < 8) a2 = wr[40 + kk]; else a2 = aq[kk & 1];
        acc[0] = mfma32(wr[kk], b, acc[0]);
        acc[1] = mfma32(wr[20 + kk], b, acc[1]);
        acc[2] = mfma32(a2, b, acc[2]);
        if (kk + 3 < KS) bq[kk % 3] = ldsr4(memrd + (kk + 3) * 512);
        if (kk + 2 >= 8 && kk + 2 < KS) aq[kk & 1] = ldsr4(wl + (kk + 2 - 8) * 1024);
        SCHED_FENCE();
      }
    }
#pragma unroll
    for (int t = 0; t < 3; ++t) {
      const int tau = 3 * w + t;
      v2u o = {pack2(acc[t][0] * qs, acc[t][1] * qs), pack2(acc[t][2] * qs, acc[t][3] * qs)};
      *(LAS v2u*)(L + SL_QK + (tau >> 1) * 1024 + lane * 16 + (tau & 1) * 8) = o;
    }
    STAMP(h, s, 3);
    RAW_BARRIER();
    STAMP(h, s, 4);
    if (w == 0) {
      LAS unsigned char* kx = L + SL_KX + (s & 1) * 12288 + lane * 16;
      LAS unsigned char* qk = L + SL_QK + lane * 16;
      f32x4 sm = {0, 0, 0, 0}, s0 = {0, 0, 0, 0}, s1 = {0, 0, 0, 0};
      v4u fq[2], fm[2], f0[2], f1[2];
      fq[0] = ldsr4(qk); fm[0] = ldsr4(qk + 6 * 1024); f0[0] = ldsr4(kx); f1[0] = ldsr4(kx + 6 * 1024);
      fq[1] = ldsr4(qk + 1024); fm[1] = ldsr4(qk + 7 * 1024); f0[1] = ldsr4(kx + 1024); f1[1] = ldsr4(kx + 7 * 1024);
      SCHED_FENCE();
#pragma unroll
      for (int p = 0; p < 6; ++p) {
        const int b = p & 1;
        sm = mfma32(fm[b], fq[b], sm);
        s0 = mfma32(f0[b], fq[b], s0);
        s1 = mfma32(f1[b], fq[b], s1);
        if (p + 2 < 6) {
          fq[b] = ldsr4(qk + (p + 2) * 1024); fm[b] = ldsr4(qk + (8 + p) * 1024);
          f0[b] = ldsr4(kx + (p + 2) * 1024); f1[b] = ldsr4(kx + (8 + p) * 1024);
        }
        SCHED_FENCE();
      }
      const float NEG = -3.0e38f;
      const bool up = n >= 8;
      float v[6];
      v[0] = up ? s1[0] : s0[0]; v[1] = up ? s1[1] : s0[1]; v[2] = up ? s1[2] : s0[2]; v[3] = up ? s1[3] : s0[3];
      v[4] = up ? sm[2] : sm[0]; v[5] = up ? sm[3] : sm[1];
      if (g >= 2) { v[4] = NEG; v[5] = NEG; }
      float mx = xmax8(max2f(max3f(v[0], v[1], v[2]), max3f(v[3], v[4], v[5])));
      float sum = 0.f;
#pragma unroll
      for (int r = 0; r < 6; ++r) { v[r] = __builtin_amdgcn_exp2f(v[r] - mx); sum += v[r]; }
      const float inv = __builtin_amdgcn_rcpf(xsum8(sum));
      v2u px = {nosent(pack2(v[0] * inv, v[1] * inv)), nosent(pack2(v[2] * inv, v[3] * inv))};
      const unsigned pm = nosent(pack2(v[4] * inv, v[5] * inv));
      __builtin_amdgcn_raw_buffer_store_b64(px, rs, pox, soff, AUX_SC1);
      if (g < 2) __builtin_amdgcn_raw_buffer_store_b32(pm, rs, pom, soff, AUX_SC1);
      STAMP(h, s, 5);
    }
    if (s + 1 < NSEG - 1) { if (!fetch_kx(s + 1)) return; }
  }
}

__device__ __forceinline__ void col_role(unsigned char* __restrict__ ws, __amdgpu_buffer_rsrc_t rs, int c8, LAS unsigned char* L) {
  const int tid = threadIdx.x, lane = tid & 63, g = lane >> 4, n = lane & 15;
  const int w = __builtin_amdgcn_readfirstlane(tid >> 6);
  v4u wr[40];
  {
    const unsigned char* src = ws + OFF_WVF + ((size_t)(c8 * 16 + 2 * w) * 20) * 1024 + lane * 16;
#pragma unroll
    for (int i = 0; i < 40; ++i) wr[i] = *(const v4u*)(src + i * 1024);
  }
  auto stage_x = [&](int seg) {
    const unsigned char* src = ws + OFF_XNF + (size_t)seg * 40960 + w * 5120 + lane * 16;
    LAS unsigned char* dst = L + CL_X + (seg & 1) * 40960 + w * 5120;
#pragma unroll
    for (int i = 0; i < 5; ++i)
      __builtin_amdgcn_global_load_lds((const GAS void*)(src + i * 1024), (LAS void*)(dst + i * 1024), 16, 0, 0);
  };
  stage_x(0);
  stage_x(1);
  __syncthreads();
  v4u vox[2]; v2u vom[2];
  auto compute_vox = [&](int seg) {
    LAS unsigned char* xr = L + CL_X + (seg & 1) * 40960 + lane * 16;
    f32x4 a00 = {0, 0, 0, 0}, a01 = {0, 0, 0, 0}, a10 = {0, 0, 0, 0}, a11 = {0, 0, 0, 0};
    v4u x0[2], x1[2];
    x0[0] = ldsr4(xr); x1[0] = ldsr4(xr + 20 * 1024); x0[1] = ldsr4(xr + 1024); x1[1] = ldsr4(xr + 21 * 1024);
    SCHED_FENCE();
#pragma unroll
    for (int kk = 0; kk < KS; ++kk) {
      const int b = kk & 1;
      a00 = mfma32(x0[b], wr[kk], a00); a01 = mfma32(x1[b], wr[kk], a01);
      a10 = mfma32(x0[b], wr[20 + kk], a10); a11 = mfma32(x1[b], wr[20 + kk], a11);
      if (kk + 2 < KS) { x0[b] = ldsr4(xr + (kk + 2) * 1024); x1[b] = ldsr4(xr + (22 + kk) * 1024); }
      SCHED_FENCE();
    }
    vox[0] = v4u{pack2(a00[0], a00[1]), pack2(a00[2], a00[3]), pack2(a01[0], a01[1]), pack2(a01[2], a01[3])};
    vox[1] = v4u{pack2(a10[0], a10[1]), pack2(a10[2], a10[3]), pack2(a11[0], a11[1]), pack2(a11[2], a11[3])};
  };
  compute_vox(0);
  unsigned pol = 0;
  LAS unsigned char* memrd = L + CL_MEM + (g * 8 + (n & 7)) * 16;
  const int oa = xmap(10240 + (4 * w + (n >> 3)) * 640 + (g * 8 + (n & 7)) * 16);
  const int ob = xmap(10240 + (4 * w + 2 + (n >> 3)) * 640 + (g * 8 + (n & 7)) * 16);
  const int om = xmap(10240 + (4 * w + 2 * (g >> 1) + (n >> 3)) * 640 + 512 + ((g & 1) * 8 + (n & 7)) * 8);
  const int opub = xmap(c8 * 128 + n * 8);
  for (int s = 0; s < NSEG; ++s) {
    if (s == POL_FROM - 1) pol = wait_pol(rs);
    const int soff = slot_off(s, pol);
    STAMP(32 + c8, s, 0);
    if (!gather_mem(rs, soff, w, lane, L + CL_MEM)) return;
    __syncthreads();
    STAMP(32 + c8, s, 1);
    if (s >= 1 && s + 1 < NSEG) stage_x(s + 1);
    if (c8 == 0 && tid == 64) __builtin_amdgcn_raw_buffer_store_b32((unsigned)(s + 1), rs, XB_PROG, 0, AUX_SC1);
    {
      f32x4 m0 = {0, 0, 0, 0}, m1 = {0, 0, 0, 0};
      v4u aq[3];
      aq[0] = ldsr4(memrd); aq[1] = ldsr4(memrd + 512); aq[2] = ldsr4(memrd + 1024);
      SCHED_FENCE();
#pragma unroll
      for (int kk = 0; kk < KS; ++kk) {
        const v4u a = aq[kk % 3];
        m0 = mfma32(a, wr[kk], m0); m1 = mfma32(a, wr[20 + kk], m1);
        if (kk + 3 < KS) aq[kk % 3] = ldsr4(memrd + (kk + 3) * 512);
        SCHED_FENCE();
      }
      vom[0] = v2u{pack2(m0[0], m0[1]), pack2(m0[2], m0[3])};
      vom[1] = v2u{pack2(m1[0], m1[1]), pack2(m1[2], m1[3])};
    }
    {
      unsigned char* dst = ws + OFF_VO + ((size_t)(s * 80 + c8) * 16 + 2 * w) * 1280;
#pragma unroll
      for (int tt = 0; tt < 2; ++tt) {
        *(v4u*)(dst + tt * 1280 + lane * 16) = vox[tt];
        if (g < 2) *(v2u*)(dst + tt * 1280 + 1024 + lane * 8) = vom[tt];
      }
    }
    if (s == NSEG - 1) break;
    STAMP(32 + c8, s, 2);
    v4u pa, pb; v2u pmv;
    for (unsigned spins = 0;; ++spins) {
      asm volatile("" ::: "memory");
      pa = __builtin_amdgcn_raw_buffer_load_b128(rs, oa, soff, AUX_SC1);
      pb = __builtin_amdgcn_raw_buffer_load_b128(rs, ob, soff, AUX_SC1);
      pmv = __builtin_amdgcn_raw_buffer_load_b64(rs, om, soff, AUX_SC1);
      if (__all(valid4(pa) & valid4(pb) & valid2(pmv))) break;
      if (spins > SPIN_CAP) { give_up(rs); return; }
    }
    STAMP(32 + c8, s, 3);
    {
      const v2u z = {0u, 0u};
      const v2u pm0 = g < 2 ? pmv : z, pm1 = g < 2 ? z : pmv;
      f32x4 d0 = {0, 0, 0, 0}, d1 = {0, 0, 0, 0};
      d0 = mfma32(vox[0], pa, d0); d1 = mfma32(vox[1], pb, d1);
      d0 = mfma16(vom[0], pm0, d0); d1 = mfma16(vom[1], pm1, d1);
      d0 += d1;
      if ((g >> 1) == (n >> 3)) *(LAS f32x4*)(L + CL_PART + w * 512 + (n >> 3) * 256 + (n & 7) * 32 + (g & 1) * 16) = d0;
    }
    RAW_BARRIER();
    STAMP(32 + c8, s, 4);
    if (w == 0) {
      LAS unsigned char* pr = L + CL_PART + g * 1024 + n * 16;
      f32x4 t = (*(LAS f32x4*)(pr) + *(LAS f32x4*)(pr + 256)) + (*(LAS f32x4*)(pr + 512) + *(LAS f32x4*)(pr + 768));
      t[0] = xsum4(t[0]); t[1] = xsum4(t[1]); t[2] = xsum4(t[2]); t[3] = xsum4(t[3]);
      v2u o = {nosent(pack2(t[0], t[1])), nosent(pack2(t[2], t[3]))};
      if (g == 0) __builtin_amdgcn_raw_buffer_store_b64(o, rs, opub, slot_off(s + 1, pol), AUX_SC1);
    }
    STAMP(32 + c8, s, 5);
    SCHED_FENCE();
    RAW_BARRIER();
    compute_vox(s + 1);
    STAMP(32 + c8, s, 6);
  }
}

__device__ __forceinline__ void lds_sync6(volatile LAS unsigned* cnt, unsigned target, int lane) {
  asm volatile("s_waitcnt lgkmcnt(0)" ::: "memory");
  if (lane == 0) __hip_atomic_fetch_add((LAS unsigned*)cnt, 1u, __ATOMIC_RELAXED, __HIP_MEMORY_SCOPE_WORKGROUP);
  while (*cnt < target) __builtin_amdgcn_s_sleep(1);
  asm volatile("" ::: "memory");
}

__device__ __forceinline__ void xs_role(unsigned char* __restrict__ ws, __amdgpu_buffer_rsrc_t rs, int h, int sp, LAS unsigned char* L) {
  const int tid = threadIdx.x, lane = tid & 63, g = lane >> 4, n = lane & 15;
  const int w = __builtin_amdgcn_readfirstlane(tid >> 6);
  const __amdgpu_buffer_rsrc_t rq = __builtin_amdgcn_make_buffer_rsrc((void*)(ws + OFF_QX), 0, (int)(OFF_PX - OFF_QX), 0x00020000);
  constexpr int RO_KX = (int)(OFF_KX - OFF_QX), RO_KM = (int)(OFF_KM - OFF_QX);
  volatile LAS unsigned* cnt = (volatile LAS unsigned*)(L + XL_CNT);
  const bool isk = w < 3;
  const int pl = isk ? w : (w == 3 ? 2 : (w - 4) & 1);
  v4u wr[40];
  if (w < 6) {
    const unsigned char* src = ws + OFF_WF + ((size_t)(h * 24 + (isk ? 12 : 0) + 6 * sp + 2 * pl) * 20) * 1024 + lane * 16;
#pragma unroll
    for (int i = 0; i < 40; ++i) wr[i] = *(const v4u*)(src + i * 1024);
  } else {
#pragma unroll
    for (int i = 0; i < 40; ++i) wr[i] = v4u{0u, 0u, 0u, 0u};
  }
  if (tid == 0) cnt[0] = 0u;
  auto stage_x = [&](int seg) {
    const unsigned char* src = ws + OFF_XNF + (size_t)seg * 40960 + (w - 6) * 20480 + lane * 16;
    LAS unsigned char* dst = L + XL_X + (seg & 1) * 40960 + (w - 6) * 20480;
#pragma unroll
    for (int i = 0; i < 20; ++i)
      __builtin_amdgcn_global_load_lds((const GAS void*)(src + i * 1024), (LAS void*)(dst + i * 1024), 16, 0, 0);
  };
  if (w >= 6) stage_x(0);
  asm volatile("s_waitcnt vmcnt(0)" ::: "memory");
  const float sc = isk ? 1.f : QSCALE;
  for (int s = 0; s < NSEG + 2; ++s) {
    RAW_BARRIER();
    if (s >= 2 && tid == 448) __builtin_amdgcn_raw_buffer_store_b32(1u, rs, XB_XFLAG + ((s - 2) * 32 + h) * 64 + sp * 4, 0, AUX_SC1);
    if (w >= 6) {
      if (s + 1 < NSEG) stage_x(s + 1);
      asm volatile("s_waitcnt vmcnt(0)" ::: "memory");
    } else if (s < NSEG) {
      LAS unsigned char* xr = L + XL_X + (s & 1) * 40960 + lane * 16;
      f32x4 a00 = {0, 0, 0, 0}, a01 = {0, 0, 0, 0}, a10 = {0, 0, 0, 0}, a11 = {0, 0, 0, 0};
      v4u x0[2], x1[2];
      x0[0] = ldsr4(xr); x1[0] = ldsr4(xr + 20 * 1024); x0[1] = ldsr4(xr + 1024); x1[1] = ldsr4(xr + 21 * 1024);
      SCHED_FENCE();
#pragma unroll
      for (int kk = 0; kk < KS; ++kk) {
        const int b = kk & 1;
        a00 = mfma32(wr[kk], x0[b], a00); a01 = mfma32(wr[kk], x1[b], a01);
        a10 = mfma32(wr[20 + kk], x0[b], a10); a11 = mfma32(wr[20 + kk], x1[b], a11);
        if (kk + 2 < KS) { x0[b] = ldsr4(xr + (kk + 2) * 1024); x1[b] = ldsr4(xr + (22 + kk) * 1024); }
        SCHED_FENCE();
      }
      const v4u o0 = {pack2(a00[0] * sc, a00[1] * sc), pack2(a00[2] * sc, a00[3] * sc), pack2(a10[0] * sc, a10[1] * sc), pack2(a10[2] * sc, a10[3] * sc)};
      const v4u o1 = {pack2(a01[0] * sc, a01[1] * sc), pack2(a01[2] * sc, a01[3] * sc), pack2(a11[0] * sc, a11[1] * sc), pack2(a11[2] * sc, a11[3] * sc)};
      const int dst = (isk ? RO_KX : 0) + ((s * 32 + h) * 12 + 3 * sp + pl) * 1024 + lane * 16;
      __builtin_amdgcn_raw_buffer_store_b128(o0, rq, dst, 0, AUX_SC1);
      __builtin_amdgcn_raw_buffer_store_b128(o1, rq, dst + 6 * 1024, 0, AUX_SC1);
      asm volatile("s_waitcnt vmcnt(2)" ::: "memory");
    } else {
      asm volatile("s_waitcnt vmcnt(0)" ::: "memory");
    }
  }
  const unsigned pol = wait_pol(rs);
  if (w < 6) {
    LAS unsigned char* memrd = L + XL_MEM + (g * 8 + (n & 7)) * 16;
    const int p0 = 128 * w + lane, o0 = xmap(p0 * 16), o1 = xmap(p0 * 16 + 1024);
    for (int s = 0; s < NSEG; ++s) {
      if (w < 5) {
        for (unsigned spins = 0;; ++spins) {
          asm volatile("" ::: "memory");
          const unsigned pr = __builtin_amdgcn_raw_buffer_load_b32(rs, XB_PROG, 0, AUX_SC1);
          if (pr != SENT && pr > (unsigned)s) break;
          if (spins > SPIN_CAP) { give_up(rs); break; }
          __builtin_amdgcn_s_sleep(16);
        }
        const int soff = slot_off(s, pol);
        v4u v0, v1;
        for (unsigned spins = 0;; ++spins) {
          asm volatile("" ::: "memory");
          v0 = __builtin_amdgcn_raw_buffer_load_b128(rs, o0, soff, AUX_SC1);
          v1 = __builtin_amdgcn_raw_buffer_load_b128(rs, o1, soff, AUX_SC1);
          if (__all(valid4(v0) & valid4(v1))) break;
          if (spins > SPIN_CAP) { give_up(rs); break; }
          __builtin_amdgcn_s_sleep(8);
        }
        *(LAS v4u*)(L + XL_MEM + p0 * 16) = v0;
        *(LAS v4u*)(L + XL_MEM + p0 * 16 + 1024) = v1;
      }
      lds_sync6(cnt, (unsigned)(12 * s + 6), lane);
      if (isk) {
        f32x4 m0 = {0, 0, 0, 0}, m1 = {0, 0, 0, 0};
        v4u aq[3];
        aq[0] = ldsr4(memrd); aq[1] = ldsr4(memrd + 512); aq[2] = ldsr4(memrd + 1024);
        SCHED_FENCE();
#pragma unroll
        for (int kk = 0; kk < KS; ++kk) {
          const v4u b = aq[kk % 3];
          m0 = mfma32(wr[kk], b, m0); m1 = mfma32(wr[20 + kk], b, m1);
          if (kk + 3 < KS) aq[kk % 3] = ldsr4(memrd + (kk + 3) * 512);
          SCHED_FENCE();
        }
        const v4u o = {pack2(m0[0], m0[1]), pack2(m0[2], m0[3]), pack2(m1[0], m1[1]), pack2(m1[2], m1[3])};
        __builtin_amdgcn_raw_buffer_store_b128(o, rq, RO_KM + ((s * 32 + h) * 6 + 3 * sp + pl) * 1024 + lane * 16, 0, AUX_SC1);
        asm volatile("s_waitcnt vmcnt(0)" ::: "memory");
      }
      lds_sync6(cnt, (unsigned)(12 * s + 12), lane);
      if (tid == 0) __builtin_amdgcn_raw_buffer_store_b32(1u, rs, XB_KMFLAG + (s * 32 + h) * 64 + sp * 4, 0, AUX_SC1);
    }
  } else {
    const int rt = sp;
    for (int s = (w & 1); s < NSEG; s += 2) {
      if (!wait_flags2(rs, XB_XFLAG + (s * 32 + h) * 64)) return;
      const int qo = ((s * 32 + h) * 12 + rt * 6) * 1024 + lane * 16;
      const int ko = RO_KX + ((s * 32 + h) * 12) * 1024 + lane * 16;
      v4u fq[6], f0[6], f1[6], fm[6];
#pragma unroll
      for (int p = 0; p < 6; ++p) {
        fq[p] = __builtin_amdgcn_raw_buffer_load_b128(rq, qo + p * 1024, 0, AUX_SC1);
        f0[p] = __builtin_amdgcn_raw_buffer_load_b128(rq, ko + p * 1024, 0, AUX_SC1);
        f1[p] = __builtin_amdgcn_raw_buffer_load_b128(rq, ko + (6 + p) * 1024, 0, AUX_SC1);
      }
      f32x4 sm = {0, 0, 0, 0}, s0 = {0, 0, 0, 0}, s1 = {0, 0, 0, 0};
#pragma unroll
      for (int p = 0; p < 6; ++p) { s0 = mfma32(f0[p], fq[p], s0); s1 = mfma32(f1[p], fq[p], s1); }
      {
        const unsigned need = (unsigned)(12 * (s + 1 < NSEG ? s + 1 : s) + 12);
        unsigned spins = 0;
        while (*cnt < need) { __builtin_amdgcn_s_sleep(8); if (++spins > SPIN_CAP) break; }
      }
      if (!wait_flags2(rs, XB_KMFLAG + (s * 32 + h) * 64)) return;
      const int mo = RO_KM + ((s * 32 + h) * 6) * 1024 + lane * 16;
#pragma unroll
      for (int p = 0; p < 6; ++p) fm[p] = __builtin_amdgcn_raw_buffer_load_b128(rq, mo + p * 1024, 0, AUX_SC1);
#pragma unroll
      for (int p = 0; p < 6; ++p) sm = mfma32(fm[p], fq[p], sm);
      const float NEG = -3.0e38f;
      const int t = rt * 16 + n;
      if (g >= 2) sm = f32x4{NEG, NEG, NEG, NEG};
#pragma unroll
      for (int r = 0; r < 4; ++r) {
        if (4 * g + r > t) s0[r] = NEG;
        if (16 + 4 * g + r > t) s1[r] = NEG;
      }
      float mx = max3f(max3f(sm[0], sm[1], sm[2]), max3f(sm[3], s0[0], s0[1]), max3f(max3f(s0[2], s0[3], s1[0]), s1[1], max2f(s1[2], s1[3])));
      mx = xmax4(mx);
      float em[4], e0[4], e1[4], sum = 0.f;
#pragma unroll
      for (int r = 0; r < 4; ++r) {
        em[r] = __builtin_amdgcn_exp2f(sm[r] - mx); e0[r] = __builtin_amdgcn_exp2f(s0[r] - mx); e1[r] = __builtin_amdgcn_exp2f(s1[r] - mx);
        sum += em[r] + e0[r] + e1[r];
      }
      const float inv = 1.f / xsum4(sum);
      const v4u px = {pack2(e0[0] * inv, e0[1] * inv), pack2(e0[2] * inv, e0[3] * inv), pack2(e1[0] * inv, e1[1] * inv), pack2(e1[2] * inv, e1[3] * inv)};
      const v2u pm = {pack2(em[0] * inv, em[1] * inv), pack2(em[2] * inv, em[3] * inv)};
      const int rg = 2 * rt + (n >> 3), l2 = g * 16 + (h & 1) * 8 + (n & 7);
      unsigned char* dst = ws + OFF_PX + ((size_t)(s * 16 + (h >> 1)) * 4 + rg) * 1536;
      *(v4u*)(dst + l2 * 16) = px;
      *(v2u*)(dst + 1024 + l2 * 8) = pm;
    }
  }
}

__global__ void __launch_bounds__(CH_T, 2) k_chain(unsigned char* __restrict__ ws, unsigned char* __restrict__ xb) {
  extern __shared__ __attribute__((aligned(16))) unsigned char smem[];
  LAS unsigned char* L = (LAS unsigned char*)smem;
  const __amdgpu_buffer_rsrc_t rs = __builtin_amdgcn_make_buffer_rsrc((void*)xb, 0, XB_SIZE, 0x00020000);
  if (threadIdx.x == 0) {
    GAS unsigned* cA = (GAS unsigned*)(xb + XB_CNTA); GAS unsigned* cB = (GAS unsigned*)(xb + XB_CNTB);
    GAS unsigned* cAll = (GAS unsigned*)(xb + XB_CNTALL); GAS unsigned* cF = (GAS unsigned*)(xb + XB_CNTF);
    unsigned xcc; asm volatile("s_getreg_b32 %0, hwreg(HW_REG_XCC_ID)" : "=s"(xcc));
    int role = -1;
    if ((xcc & 0xf) < 4) {
      const unsigned t = __hip_atomic_fetch_add(cA, 1u, __ATOMIC_RELAXED, __HIP_MEMORY_SCOPE_AGENT) + 1u;
      if (t <= (unsigned)NCH) role = (int)t;
    }
    if (role < 0) {
      const unsigned t = __hip_atomic_fetch_add(cB, 1u, __ATOMIC_RELAXED, __HIP_MEMORY_SCOPE_AGENT) + 1u;
      if (t < (unsigned)NXS) role = NCH + 1 + (int)t;
    }
    asm volatile("s_waitcnt vmcnt(0)" ::: "memory");
    __hip_atomic_fetch_add(cAll, 1u, __ATOMIC_RELAXED, __HIP_MEMORY_SCOPE_AGENT);
    if (role < 0) {
      for (unsigned spins = 0; spins < SPIN_CAP; ++spins) {
        const unsigned na = __hip_atomic_load(cA, __ATOMIC_RELAXED, __HIP_MEMORY_SCOPE_AGENT) + 1u;
        if (na > (unsigned)NCH) break;
        const unsigned nall = __hip_atomic_load(cAll, __ATOMIC_RELAXED, __HIP_MEMORY_SCOPE_AGENT) + 1u;
        if (nall >= (unsigned)CH_GRID) {
          const unsigned na2 = __hip_atomic_load(cA, __ATOMIC_RELAXED, __HIP_MEMORY_SCOPE_AGENT) + 1u;
          if (na2 <= (unsigned)NCH) {
            const unsigned t = __hip_atomic_fetch_add(cF, 1u, __ATOMIC_RELAXED, __HIP_MEMORY_SCOPE_AGENT) + 1u;
            if (na2 + t <= (unsigned)NCH) role = (int)(na2 + t);
          }
          break;
        }
        __builtin_amdgcn_s_sleep(8);
      }
    }
    *(LAS int*)(L + SL_ROLE) = role;
  }
  __syncthreads();
  const int role = __builtin_amdgcn_readfirstlane(*(LAS int*)(L + SL_ROLE));
  if (role < 0) return;
  if (role < NCH) {
    const int grp = role / 7, r = role % 7;
    if (r < 2) score_role(ws, rs, grp * 2 + r, L);
    else col_role(ws, rs, grp * 5 + r - 2, L);
  } else if (role == NCH) {
    if (threadIdx.x < 64) {
      int best[4] = {0x7fffffff, 0x7fffffff, 0x7fffffff, 0x7fffffff};
      for (int it = 0; it < 7; ++it) {
#pragma unroll
        for (int c = 0; c < 4; ++c) {
          const int off = (c >> 1) * 32 * 65536 + xmap(31 * 1024) + (c & 1) * 8192;
          const long long t0 = __builtin_amdgcn_s_memtime();
          __builtin_amdgcn_raw_buffer_store_b32((unsigned)it, rs, off, 0, AUX_SC1);
          asm volatile("s_waitcnt vmcnt(0)" ::: "memory");
          unsigned v = __builtin_amdgcn_raw_buffer_load_b32(rs, off, 0, AUX_SC1);
          asm volatile("s_waitcnt vmcnt(0)" : "+v"(v) :: "memory");
          const int dt = (int)(__builtin_amdgcn_s_memtime() - t0);
          best[c] = dt < best[c] ? dt : best[c];
        }
      }
      const unsigned pv = (best[1] < best[0] ? 1u : 0u) | (best[3] < best[2] ? 2u : 0u);
      if (threadIdx.x == 0) __builtin_amdgcn_raw_buffer_store_b32(pv, rs, XB_POL, 0, AUX_SC1);
    }
  } else {
    xs_role(ws, rs, (role - NCH - 1) >> 1, (role - NCH - 1) & 1, L);
  }
}

__global__ void __launch_bounds__(640) k_attnout(const u16* __restrict__ x, unsigned char* __restrict__ ws) {
  extern __shared__ __attribute__((aligned(16))) unsigned char smem[];
  LAS unsigned char* L = (LAS unsigned char*)smem;
  const int tid = threadIdx.x, lane = tid & 63, g = lane >> 4, n = lane & 15;
  const int w = __builtin_amdgcn_readfirstlane(tid >> 6);
  const int s = blockIdx.x >> 2, qt = blockIdx.x & 3;
  {
    const unsigned char* src = ws + OFF_PX + (size_t)s * 98304;
    for (int i = w; i < 96; i += 10)
      __builtin_amdgcn_global_load_lds((const GAS void*)(src + i * 1024 + lane * 16), (LAS void*)(L + i * 1024), 16, 0, 0);
  }
  __syncthreads();
  const int c0 = qt * 20 + 2 * w;
  f32x4 acc[2][4];
#pragma unroll
  for (int c = 0; c < 2; ++c)
#pragma unroll
    for (int r = 0; r < 4; ++r) acc[c][r] = f32x4{0, 0, 0, 0};
  const unsigned char* vsrc = ws + OFF_VO + ((size_t)(s * 80 + c0) * 16) * 1280;
#pragma unroll 2
  for (int hp = 0; hp < 16; ++hp) {
    v4u vx[2]; v2u vm[2];
#pragma unroll
    for (int c = 0; c < 2; ++c) {
      vx[c] = *(const v4u*)(vsrc + (size_t)(c * 16 + hp) * 1280 + lane * 16);
      vm[c] = *(const v2u*)(vsrc + (size_t)(c * 16 + hp) * 1280 + 1024 + (lane & 31) * 8);
    }
#pragma unroll
    for (int rg = 0; rg < 4; ++rg) {
      const v4u px = *(LAS v4u*)(L + (hp * 4 + rg) * 1536 + lane * 16);
      const v2u pm = *(LAS v2u*)(L + (hp * 4 + rg) * 1536 + 1024 + lane * 8);
#pragma unroll
      for (int c = 0; c < 2; ++c) {
        acc[c][rg] = mfma32(vx[c], px, acc[c][rg]);
        acc[c][rg] = mfma16(vm[c], pm, acc[c][rg]);
      }
    }
  }
  float* hout = (float*)(ws + OFF_H);
#pragma unroll
  for (int c = 0; c < 2; ++c)
#pragma unroll
    for (int rg = 0; rg < 4; ++rg) {
      f32x4 o;
#pragma unroll
      for (int r = 0; r < 4; ++r) o[r] = acc[c][rg][r] + __shfl(acc[c][rg][r], (lane + 40) & 63);
      if (g < 2 && n < 8) {
        const int t = s * 32 + rg * 8 + n, col = (c0 + c) * 8 + g * 4;
        const v2u xv = *(const v2u*)(x + (size_t)t * DM + col);
        o[0] += bflo(xv.x); o[1] += bfhi(xv.x); o[2] += bflo(xv.y); o[3] += bfhi(xv.y);
        *(f32x4*)(hout + (size_t)t * DM + col) = o;
      }
    }
}

__global__ void __launch_bounds__(256) k_ln2(unsigned char* __restrict__ ws) {
  const int t = threadIdx.x, lane = t & 63, w = t >> 6;
  const int row = blockIdx.x * 16 + w * 4 + (lane >> 4), q = lane & 15;
  const float* hsrc = (const float*)(ws + OFF_H) + (size_t)row * DM;
  float v[5][8];
  float s = 0.f;
#pragma unroll
  for (int i = 0; i < 5; ++i) {
    f32x4 a = *(const f32x4*)(hsrc + (q + 16 * i) * 8), b = *(const f32x4*)(hsrc + (q + 16 * i) * 8 + 4);
#pragma unroll
    for (int j = 0; j < 4; ++j) { v[i][j] = a[j]; v[i][4 + j] = b[j]; s += a[j] + b[j]; }
  }
#pragma unroll
  for (int o = 1; o < 16; o <<= 1) s += __shfl_xor(s, o);
  const float mean = s * (1.f / DM);
  float qq = 0.f;
#pragma unroll
  for (int i = 0; i < 5; ++i)
#pragma unroll
    for (int j = 0; j < 8; ++j) { float d = v[i][j] - mean; qq += d * d; }
#pragma unroll
  for (int o = 1; o < 16; o <<= 1) qq += __shfl_xor(qq, o);
  const float rs = 1.f / sqrtf(qq * (1.f / DM) + 1e-5f);
  unsigned char* outF = ws + OFF_HNF;
#pragma unroll
  for (int i = 0; i < 5; ++i) {
    const int pc = q + 16 * i, kk = pc >> 2, g = pc & 3;
    v4u o;
    o.x = pack2((v[i][0] - mean) * rs, (v[i][1] - mean) * rs);
    o.y = pack2((v[i][2] - mean) * rs, (v[i][3] - mean) * rs);
    o.z = pack2((v[i][4] - mean) * rs, (v[i][5] - mean) * rs);
    o.w = pack2((v[i][6] - mean) * rs, (v[i][7] - mean) * rs);
    *(v4u*)(outF + ((size_t)(row >> 4) * 20 + kk) * 1024 + (g * 16 + (row & 15)) * 16) = o;
  }
}

__device__ __forceinline__ float gelu_tanh(float x) {
  const float u = 0.7978845608028654f * (x + 0.044715f * x * x * x);
  const float e = __expf(2.f * u);
  const float th = 1.f - 2.f / (e + 1.f);
  return 0.5f * x * (1.f + th);
}
__global__ void __launch_bounds__(256) k_ffn1(unsigned char* __restrict__ ws) {
  const int lane = threadIdx.x & 63;
  const int bw = blockIdx.x;
  const int rgp = bw & 15, fp = bw >> 4;
  const int rt0 = (rgp * 4 + (threadIdx.x >> 6)) * 2;
  const unsigned char* wsrc = ws + OFF_WGU + ((size_t)(fp * 2) * 2 * 20) * 1024 + lane * 16;
  const unsigned char* xsrc = ws + OFF_HNF + ((size_t)rt0 * 20) * 1024 + lane * 16;
  f32x4 acc[4][2];
#pragma unroll
  for (int i = 0; i < 4; ++i) { acc[i][0] = f32x4{0, 0, 0, 0}; acc[i][1] = f32x4{0, 0, 0, 0}; }
#pragma unroll 2
  for (int kk = 0; kk < KS; ++kk) {
    v4u b0 = *(const v4u*)(xsrc + kk * 1024), b1 = *(const v4u*)(xsrc + (20 + kk) * 1024);
#pragma unroll
    for (int i = 0; i < 4; ++i) {
      v4u a = *(const v4u*)(wsrc + (i * 20 + kk) * 1024);
      acc[i][0] = mfma32(a, b0, acc[i][0]);
      acc[i][1] = mfma32(a, b1, acc[i][1]);
    }
  }
#pragma unroll
  for (int m = 0; m < 2; ++m) {
    float o[8];
#pragma unroll
    for (int r = 0; r < 4; ++r) {
      o[r] = gelu_tanh(acc[0][m][r]) * acc[1][m][r];
      o[4 + r] = gelu_tanh(acc[2][m][r]) * acc[3][m][r];
    }
    v4u ov = {pack2(o[0], o[1]), pack2(o[2], o[3]), pack2(o[4], o[5]), pack2(o[6], o[7])};
    *(v4u*)(ws + OFF_AF + ((size_t)(rt0 + m) * 48 + fp) * 1024 + lane * 16) = ov;
  }
}

__global__ void __launch_bounds__(256) k_ffn2(unsigned char* __restrict__ ws, u16* __restrict__ out) {
  const int lane = threadIdx.x & 63, g = lane >> 4, n = lane & 15;
  const int bw = blockIdx.x;
  const int rgp = bw & 15, cg = bw >> 4;
  const int rt0 = (rgp * 4 + (threadIdx.x >> 6)) * 2;
  const unsigned char* wsrc = ws + OFF_WD + ((size_t)(cg * 4) * 48) * 1024 + lane * 16;
  const unsigned char* xsrc = ws + OFF_AF + ((size_t)rt0 * 48) * 1024 + lane * 16;
  f32x4 acc[4][2];
#pragma unroll
  for (int i = 0; i < 4; ++i) { acc[i][0] = f32x4{0, 0, 0, 0}; acc[i][1] = f32x4{0, 0, 0, 0}; }
#pragma unroll 2
  for (int kk = 0; kk < 48; ++kk) {
    v4u b0 = *(const v4u*)(xsrc + kk * 1024), b1 = *(const v4u*)(xsrc + (48 + kk) * 1024);
#pragma unroll
    for (int i = 0; i < 4; ++i) {
      v4u a = *(const v4u*)(wsrc + (i * 48 + kk) * 1024);
      acc[i][0] = mfma32(a, b0, acc[i][0]);
      acc[i][1] = mfma32(a, b1, acc[i][1]);
    }
  }
  const float* hsrc = (const float*)(ws + OFF_H);
#pragma unroll
  for (int m = 0; m < 2; ++m)
#pragma unroll
    for (int i = 0; i < 4; ++i) {
      const int t = (rt0 + m) * 16 + n, col = (cg * 4 + i) * 16 + g * 4;
      const f32x4 hv = *(const f32x4*)(hsrc + (size_t)t * DM + col);
      v2u o = {pack2(acc[i][m][0] + hv[0], acc[i][m][1] + hv[1]), pack2(acc[i][m][2] + hv[2], acc[i][m][3] + hv[3])};
      *(v2u*)(out + (size_t)t * DM + col) = o;
    }
}

static void run(void* const* d_in, void* d_out, void* d_ws, hipStream_t st) {
  const u16* x = (const u16*)d_in[0]; const u16* mem0 = (const u16*)d_in[1];
  const u16* Wq = (const u16*)d_in[2]; const u16* Wk = (const u16*)d_in[3]; const u16* Wv = (const u16*)d_in[4]; const u16* Wo = (const u16*)d_in[5];
  const u16* wg = (const u16*)d_in[8]; const u16* wu = (const u16*)d_in[9]; const u16* wd = (const u16*)d_in[10];
  unsigned char* ws = (unsigned char*)d_ws;
  unsigned char* xb = (unsigned char*)(((uintptr_t)ws + (2u << 20) - 1) & ~(uintptr_t)((2u << 20) - 1));
  (void)hipFuncSetAttribute((const void*)k_chain, hipFuncAttributeMaxDynamicSharedMemorySize, CH_LDS);
  (void)hipFuncSetAttribute((const void*)k_attnout, hipFuncAttributeMaxDynamicSharedMemorySize, 98304);
  (void)hipMemsetAsync(xb, 0xFF, XB_SIZE, st);
  k_pre<<<PT_BLOCKS + LN_BLOCKS + 1, 256, 0, st>>>(x, mem0, Wq, Wk, Wo, wg, wu, wd, ws, xb);
  k_wvo<<<320, 256, 0, st>>>(Wv, ws);
  k_chain<<<CH_GRID, CH_T, CH_LDS, st>>>(ws, xb);
  k_attnout<<<256, 640, 98304, st>>>(x, ws);
  k_ln2<<<128, 256, 0, st>>>(ws);
  k_ffn1<<<16 * 48, 256, 0, st>>>(ws);
  k_ffn2<<<160, 256, 0, st>>>(ws, (u16*)d_out);
}
}

extern "C" void kernel_launch(void* const* d_in, const int* in_sizes, int n_in, void* d_out, int out_size, void* d_ws, size_t ws_size,
                              hipStream_t stream) {
  rmt::run(d_in, d_out, d_ws, stream);
}
```

```cpp
#include <hip/hip_runtime.h>
#include <hip/hip_bf16.h>
#include <stdint.h>

namespace rmt {
typedef unsigned short u16;
typedef unsigned v4u __attribute__((ext_vector_type(4)));
typedef unsigned v2u __attribute__((ext_vector_type(2)));
typedef float f32x4 __attribute__((ext_vector_type(4)));
typedef short bf16x8 __attribute__((ext_vector_type(8)));
typedef short bf16x4 __attribute__((ext_vector_type(4)));
typedef __bf16 bfv2 __attribute__((ext_vector_type(2)));
#define SCHED_FENCE() __builtin_amdgcn_sched_barrier(0)
#define LAS __attribute__((address_space(3)))
#define GAS __attribute__((address_space(1)))

constexpr int S = 2048, DM = 640, H = 32, DH = 192, HD = H * DH, SEG = 32, NSEG = 64, DF = 1536;
constexpr int KS = DM / 32;
constexpr unsigned SENT = 0xFFFFFFFFu;
constexpr int AUX_SC1 = 16;

constexpr int XB_SLOTS = 64 * 65536;
constexpr int XB_CTL = XB_SLOTS;
constexpr int XB_POL = XB_CTL + 128, XB_PROG = XB_CTL + 256, XB_CNTA = XB_CTL + 384, XB_CNTB = XB_CTL + 512, XB_CNTALL = XB_CTL + 640,
              XB_CNTF = XB_CTL + 768, XB_CNTJ = XB_CTL + 896;
constexpr int XB_XFLAG = XB_CTL + 1024;
constexpr int XB_KMFLAG = XB_XFLAG + 64 * 32 * 64;
constexpr int XB_VOCNT = XB_KMFLAG + 64 * 32 * 64;
constexpr int XB_PXCNT = XB_VOCNT + 64 * 128;
constexpr int XB_SIZE = XB_PXCNT + 64 * 128;
constexpr size_t OFF_XNF = (size_t)8 << 20;
constexpr size_t OFF_WF = OFF_XNF + (size_t)128 * 20 * 1024;
constexpr size_t OFF_WVF = OFF_WF + (size_t)32 * 24 * 20 * 1024;
constexpr size_t OFF_WGU = OFF_WVF + (size_t)80 * 16 * 20 * 1024;
constexpr size_t OFF_WD = OFF_WGU + (size_t)96 * 2 * 20 * 1024;
constexpr size_t OFF_QX = OFF_WD + (size_t)40 * 48 * 1024;
constexpr size_t OFF_KX = OFF_QX + (size_t)64 * 32 * 12 * 1024;
constexpr size_t OFF_KM = OFF_KX + (size_t)64 * 32 * 12 * 1024;
constexpr size_t OFF_PX = OFF_KM + (size_t)64 * 32 * 6 * 1024;
constexpr size_t OFF_VO = OFF_PX + (size_t)64 * 16 * 4 * 1536;
constexpr size_t OFF_H = OFF_VO + (size_t)64 * 80 * 16 * 1280;
constexpr size_t OFF_WOF = OFF_H;
constexpr size_t OFF_HNF = OFF_H + (size_t)S * DM * 4;
constexpr size_t OFF_AF = OFF_HNF + (size_t)128 * 20 * 1024;
constexpr size_t WS_END = OFF_AF + (size_t)128 * 48 * 1024;
static_assert(OFF_WOF + (size_t)40 * 192 * 1024 <= WS_END, "WOF alias");
static_assert((size_t)XB_SIZE + (2u << 20) <= OFF_XNF, "xb");
static_assert(WS_END <= (size_t)256 * 1024 * 1024, "workspace");

__device__ __forceinline__ float bf2f(unsigned v) { return __uint_as_float(v << 16); }
__device__ __forceinline__ float bflo(unsigned v) { return __uint_as_float(v << 16); }
__device__ __forceinline__ float bfhi(unsigned v) { return __uint_as_float(v & 0xFFFF0000u); }
__device__ __forceinline__ unsigned pack2(float a, float b) {
  bfv2 v = {(__bf16)a, (__bf16)b};
  return __builtin_bit_cast(unsigned, v);
}
__device__ __forceinline__ unsigned nosent(unsigned d) { return d == SENT ? 0x7FC07FC0u : d; }
__device__ __forceinline__ f32x4 mfma32(v4u a, v4u b, f32x4 c) {
  return __builtin_amdgcn_mfma_f32_16x16x32_bf16(__builtin_bit_cast(bf16x8, a), __builtin_bit_cast(bf16x8, b), c, 0, 0, 0);
}
__device__ __forceinline__ f32x4 mfma16(v2u a, v2u b, f32x4 c) {
  return __builtin_amdgcn_mfma_f32_16x16x16bf16_1k(__builtin_bit_cast(bf16x4, a), __builtin_bit_cast(bf16x4, b), c, 0, 0, 0);
}
__device__ __forceinline__ float xmax4(float x) {
  auto s = __builtin_amdgcn_permlane16_swap(__float_as_uint(x), __float_as_uint(x), false, false);
  x = fmaxf(__uint_as_float(s[0]), __uint_as_float(s[1]));
  auto t = __builtin_amdgcn_permlane32_swap(__float_as_uint(x), __float_as_uint(x), false, false);
  return fmaxf(__uint_as_float(t[0]), __uint_as_float(t[1]));
}
__device__ __forceinline__ float xsum4(float x) {
  auto s = __builtin_amdgcn_permlane16_swap(__float_as_uint(x), __float_as_uint(x), false, false);
  x = __uint_as_float(s[0]) + __uint_as_float(s[1]);
  auto t = __builtin_amdgcn_permlane32_swap(__float_as_uint(x), __float_as_uint(x), false, false);
  return __uint_as_float(t[0]) + __uint_as_float(t[1]);
}
__device__ __forceinline__ int xmap(int o) { return (o >> 13) * 16384 + (o & 8191); }
__device__ __forceinline__ bool valid4(v4u v) { return (v.x != SENT) & (v.y != SENT) & (v.z != SENT) & (v.w != SENT); }
__device__ __forceinline__ bool valid2(v2u v) { return (v.x != SENT) & (v.y != SENT); }

constexpr int NB_VO = 256, NB_WQ = 5 * 48, NB_WG = 5 * 12, NB_WD = 12 * 5;
constexpr int LN_BLOCKS = S / 16;
constexpr int NB_FILL = (XB_SIZE - 32768 + 65535) / 65536;
constexpr int PRE_GRID = NB_VO + 2 * NB_WQ + LN_BLOCKS + 1 + 2 * NB_WG + NB_WD + NB_FILL;

__device__ __forceinline__ void ln_rows_bf16(const u16* __restrict__ x, unsigned char* __restrict__ outF, int blk) {
  const int t = threadIdx.x, lane = t & 63, w = t >> 6;
  const int row = blk * 16 + w * 4 + (lane >> 4), q = lane & 15;
  float v[5][8];
  float s = 0.f;
#pragma unroll
  for (int i = 0; i < 5; ++i) {
    v4u d = *(const v4u*)(x + (size_t)row * DM + (q + 16 * i) * 8);
    v[i][0] = bflo(d.x); v[i][1] = bfhi(d.x); v[i][2] = bflo(d.y); v[i][3] = bfhi(d.y);
    v[i][4] = bflo(d.z); v[i][5] = bfhi(d.z); v[i][6] = bflo(d.w); v[i][7] = bfhi(d.w);
#pragma unroll
    for (int j = 0; j < 8; ++j) s += v[i][j];
  }
#pragma unroll
  for (int o = 1; o < 16; o <<= 1) s += __shfl_xor(s, o);
  const float mean = s * (1.f / DM);
  float qq = 0.f;
#pragma unroll
  for (int i = 0; i < 5; ++i)
#pragma unroll
    for (int j = 0; j < 8; ++j) { float d = v[i][j] - mean; qq += d * d; }
#pragma unroll
  for (int o = 1; o < 16; o <<= 1) qq += __shfl_xor(qq, o);
  const float rs = 1.f / sqrtf(qq * (1.f / DM) + 1e-5f);
#pragma unroll
  for (int i = 0; i < 5; ++i) {
    const int pc = q + 16 * i, kk = pc >> 2, g = pc & 3;
    v4u o;
    o.x = pack2((v[i][0] - mean) * rs, (v[i][1] - mean) * rs);
    o.y = pack2((v[i][2] - mean) * rs, (v[i][3] - mean) * rs);
    o.z = pack2((v[i][4] - mean) * rs, (v[i][5] - mean) * rs);
    o.w = pack2((v[i][6] - mean) * rs, (v[i][7] - mean) * rs);
    *(v4u*)(outF + ((size_t)(row >> 4) * 20 + kk) * 1024 + (g * 16 + (row & 15)) * 16) = o;
  }
}

__device__ __forceinline__ void pretile4(const u16* __restrict__ src, int ld, int kk0, int nb, int job, unsigned char* __restrict__ ws,
                                         LAS unsigned* tile) {
  const int t = threadIdx.x;
  {
    const int row = t >> 3, ch = t & 7;
    v4u a[4], c[4];
#pragma unroll
    for (int u = 0; u < 4; ++u) {
      const v4u* p = (const v4u*)(src + (size_t)((kk0 + u) * 32 + row) * ld + nb * 128 + ch * 16);
      a[u] = p[0]; c[u] = p[1];
    }
#pragma unroll
    for (int u = 0; u < 4; ++u) {
      LAS unsigned* d = tile + u * 2080 + row * 65 + ch * 8;
      d[0] = a[u].x; d[1] = a[u].y; d[2] = a[u].z; d[3] = a[u].w; d[4] = c[u].x; d[5] = c[u].y; d[6] = c[u].z; d[7] = c[u].w;
    }
  }
  __syncthreads();
#pragma unroll
  for (int u = 0; u < 4; ++u) {
    const LAS u16* tl = (const LAS u16*)(tile + u * 2080);
    const int kk = kk0 + u;
#pragma unroll
    for (int it = 0; it < 2; ++it) {
      const int pc = t + it * 256, i = pc >> 6, lane = pc & 63, g = lane >> 4, n = lane & 15;
      unsigned e[8];
#pragma unroll
      for (int j = 0; j < 8; ++j) {
        const int row = (job == 5) ? (16 * (j >> 2) + 4 * g + (j & 3)) : (8 * g + j);
        e[j] = tl[row * 130 + i * 16 + n];
      }
      v4u o = {e[0] | (e[1] << 16), e[2] | (e[3] << 16), e[4] | (e[5] << 16), e[6] | (e[7] << 16)};
      const int nt = nb * 8 + i;
      size_t dst;
      if (job <= 1) { const int h = nt / 12, et = nt % 12; dst = OFF_WF + ((size_t)(h * 24 + job * 12 + et) * 20 + kk) * 1024; }
      else if (job <= 4) dst = OFF_WGU + ((size_t)(nt * 2 + (job - 3)) * 20 + kk) * 1024;
      else dst = OFF_WD + ((size_t)nt * 48 + kk) * 1024;
      *(v4u*)(ws + dst + lane * 16) = o;
    }
  }
}

constexpr int PRE_LDS = 192 * 41 * 4 + 30 * 1024;
__global__ void __launch_bounds__(256) k_pre(const u16* __restrict__ x, const u16* __restrict__ mem0, const u16* __restrict__ Wq,
                                             const u16* __restrict__ Wk, const u16* __restrict__ Wv, const u16* __restrict__ Wo,
                                             const u16* __restrict__ wg, const u16* __restrict__ wu, const u16* __restrict__ wd,
                                             unsigned char* __restrict__ ws, unsigned char* __restrict__ xb) {
  __shared__ __attribute__((aligned(16))) unsigned char sm[PRE_LDS];
  LAS unsigned char* L = (LAS unsigned char*)sm;
  int b = blockIdx.x;
  const int t = threadIdx.x;
  if (b < NB_VO) {
    const int h = b >> 3, cq = b & 7;
    const int lane = t & 63, g = lane >> 4, n = lane & 15, w = __builtin_amdgcn_readfirstlane(t >> 6);
    LAS unsigned* raw = (LAS unsigned*)L;
    LAS unsigned char* bfr = L + 192 * 41 * 4;
    auto load_a = [&](int kk, v4u (&a)[12]) {
#pragma unroll
      for (int m = 0; m < 2; ++m)
#pragma unroll
        for (int ke = 0; ke < 6; ++ke)
          a[m * 6 + ke] = *(const v4u*)(Wv + (size_t)(32 * kk + 8 * (n >> 2) + 4 * m + (n & 3)) * HD + h * DH + ke * 32 + g * 8);
    };
    v4u a0[12], a1[12];
    load_a(w, a0);
    SCHED_FENCE();
    for (int pc = t; pc < 192 * 10; pc += 256) {
      const int e = pc / 10, ch = pc % 10;
      const v4u d = *(const v4u*)(Wo + (size_t)(h * DH + e) * DM + cq * 80 + ch * 8);
      LAS unsigned* dp = raw + e * 41 + ch * 4;
      dp[0] = d.x; dp[1] = d.y; dp[2] = d.z; dp[3] = d.w;
    }
    __syncthreads();
    {
      const LAS u16* tl = (const LAS u16*)raw;
      for (int pc = t; pc < 30 * 64; pc += 256) {
        const int f = pc >> 6, l2 = pc & 63, g2 = l2 >> 4, n2 = l2 & 15, ct = f / 6, ke = f % 6;
        unsigned e[8];
#pragma unroll
        for (int j = 0; j < 8; ++j) e[j] = tl[(ke * 32 + 8 * g2 + j) * 82 + ct * 16 + n2];
        *(LAS v4u*)(bfr + pc * 16) = v4u{e[0] | (e[1] << 16), e[2] | (e[3] << 16), e[4] | (e[5] << 16), e[6] | (e[7] << 16)};
      }
    }
    __syncthreads();
    const int hp = h >> 1, n2 = (h & 1) * 8 + (n & 7);
    auto body = [&](int kk, v4u (&a)[12]) {
#pragma unroll
      for (int ct = 0; ct < 5; ++ct) {
        f32x4 lo = {0.f, 0.f, 0.f, 0.f}, hi = {0.f, 0.f, 0.f, 0.f};
#pragma unroll
        for (int ke = 0; ke < 6; ++ke) {
          const v4u bf = *(LAS v4u*)(bfr + (ct * 6 + ke) * 1024 + lane * 16);
          lo = mfma32(a[ke], bf, lo); hi = mfma32(a[6 + ke], bf, hi);
        }
        const int c8 = 2 * (cq * 5 + ct) + (n >> 3);
        const v4u o = {pack2(lo[0], lo[1]), pack2(lo[2], lo[3]), pack2(hi[0], hi[1]), pack2(hi[2], hi[3])};
        *(v4u*)(ws + OFF_WVF + (((size_t)(c8 * 16 + hp) * 20 + kk) * 64 + g * 16 + n2) * 16) = o;
      }
    };
    load_a(w + 4, a1); SCHED_FENCE(); body(w, a0); SCHED_FENCE();
    load_a(w + 8, a0); SCHED_FENCE(); body(w + 4, a1); SCHED_FENCE();
    load_a(w + 12, a1); SCHED_FENCE(); body(w + 8, a0); SCHED_FENCE();
    load_a(w + 16, a0); SCHED_FENCE(); body(w + 12, a1); SCHED_FENCE();
    body(w + 16, a0);
    return;
  }
  b -= NB_VO;
  if (b < 2 * NB_WQ) {
    const int job = b >= NB_WQ; if (job) b -= NB_WQ;
    pretile4(job ? Wk : Wq, HD, (b / 48) * 4, b % 48, job, ws, (LAS unsigned*)L);
    return;
  }
  b -= 2 * NB_WQ;
  if (b < LN_BLOCKS) { ln_rows_bf16(x, ws + OFF_XNF, b); return; }
  b -= LN_BLOCKS;
  if (b < NB_WG) { pretile4(wg, DF, (b / 12) * 4, b % 12, 3, ws, (LAS unsigned*)L); return; }
  b -= NB_WG;
  if (b < NB_WG) { pretile4(wu, DF, (b / 12) * 4, b % 12, 4, ws, (LAS unsigned*)L); return; }
  b -= NB_WG;
  if (b < NB_WD) { pretile4(wd, DM, (b / 5) * 4, b % 5, 5, ws, (LAS unsigned*)L); return; }
  b -= NB_WD;
  const v4u ones = {SENT, SENT, SENT, SENT};
  if (b < NB_FILL) {
    const int base = 32768 + b * 65536;
#pragma unroll
    for (int i = 0; i < 16; ++i) { const int o = base + (i * 256 + t) * 16; if (o < XB_SIZE) *(v4u*)(xb + o) = ones; }
    return;
  }
  for (int pc = t; pc < 2048; pc += 256) {
    const int o = pc * 16;
    const int lo = (o < 8192) ? o : ((o >= 16384 && o < 24576) ? o - 8192 : -1);
    v4u d = ones;
    if (lo >= 0 && lo < 10240) {
      const int q = lo >> 4, c8 = q >> 3, i = q & 7;
      d = *(const v4u*)(mem0 + i * DM + c8 * 8);
      d.x = nosent(d.x); d.y = nosent(d.y); d.z = nosent(d.z); d.w = nosent(d.w);
    }
    *(v4u*)(xb + o) = d;
  }
}

constexpr int NSC = 32, NCOL = 80, NCH = NSC + NCOL, NXS = 64, NAO = 64, CH_T = 512, CH_GRID = 256;
constexpr unsigned SPIN_CAP = 1u << 22;
constexpr float QSCALE = 0.07216878364870322f * 1.4426950408889634f;
constexpr int SL_W = 0, SL_MEM = 96 * 1024, SL_QK = SL_MEM + 10240, SL_KX = SL_QK + 12 * 1024, SL_ROLE = SL_KX + 24 * 1024, SL_END = SL_ROLE + 64;
constexpr int CL_X = 0, CL_MEM = 80 * 1024, CL_PART = CL_MEM + 10240, CL_END = CL_PART + 4096;
constexpr int XL_X = 0, XL_MEM = 80 * 1024, XL_CNT = XL_MEM + 10240, XL_END = XL_CNT + 64;
constexpr int CH_LDS = SL_END;
static_assert(CH_LDS <= 160 * 1024 && CL_END <= SL_ROLE && XL_END <= SL_ROLE, "lds");

#define STAMP(blk, s, k) do {} while (0)
#define RAW_BARRIER() do { asm volatile("s_waitcnt lgkmcnt(0)" ::: "memory"); __builtin_amdgcn_s_barrier(); asm volatile("" ::: "memory"); } while (0)
__device__ __forceinline__ v4u ldsr4(LAS unsigned char* p) { return *(LAS v4u*)p; }
__device__ __forceinline__ float max3f(float a, float b, float c) { float r; asm("v_max3_f32 %0, %1, %2, %3" : "=v"(r) : "v"(a), "v"(b), "v"(c)); return r; }
__device__ __forceinline__ float max2f(float a, float b) { float r; asm("v_max_f32 %0, %1, %2" : "=v"(r) : "v"(a), "v"(b)); return r; }
__device__ __forceinline__ float ror8(float x) {
  return __builtin_bit_cast(float, __builtin_amdgcn_update_dpp(0, __builtin_bit_cast(int, x), 0x128, 0xf, 0xf, false));
}
__device__ __forceinline__ float xmax8(float x) {
  x = max2f(x, ror8(x));
  auto s = __builtin_amdgcn_permlane16_swap(__float_as_uint(x), __float_as_uint(x), false, false);
  x = max2f(__uint_as_float(s[0]), __uint_as_float(s[1]));
  auto t = __builtin_amdgcn_permlane32_swap(__float_as_uint(x), __float_as_uint(x), false, false);
  return max2f(__uint_as_float(t[0]), __uint_as_float(t[1]));
}
__device__ __forceinline__ float xsum8(float x) { x += ror8(x); return xsum4(x); }
__device__ __forceinline__ void give_up(__amdgpu_buffer_rsrc_t rs) { __builtin_amdgcn_raw_buffer_store_b32(1u, rs, XB_CTL, 0, AUX_SC1); }
constexpr int POL_FROM = 4;
__device__ __forceinline__ int slot_off(int s, unsigned pol) { return s * 65536 + (s >= POL_FROM ? (int)((pol >> (s >> 5)) & 1u) * 8192 : 0); }

__device__ __forceinline__ bool gather_mem(__amdgpu_buffer_rsrc_t rs, int soff, int w, int lane, LAS unsigned char* img) {
  const int p0 = 80 * w + lane, p1 = 80 * w + 64 + (lane & 15);
  const int o0 = xmap(p0 * 16), o1 = xmap(p1 * 16);
  v4u v0, v1;
  for (unsigned spins = 0;; ++spins) {
    asm volatile("" ::: "memory");
    v0 = __builtin_amdgcn_raw_buffer_load_b128(rs, o0, soff, AUX_SC1);
    v1 = __builtin_amdgcn_raw_buffer_load_b128(rs, o1, soff, AUX_SC1);
    if (__all(valid4(v0) & valid4(v1))) break;
    if (spins > SPIN_CAP) { give_up(rs); return false; }
  }
  *(LAS v4u*)(img + p0 * 16) = v0;
  if (lane < 16) *(LAS v4u*)(img + p1 * 16) = v1;
  return true;
}
__device__ __forceinline__ bool wait_flags2(__amdgpu_buffer_rsrc_t rs, int off) {
  for (unsigned spins = 0;; ++spins) {
    asm volatile("" ::: "memory");
    v2u f = __builtin_amdgcn_raw_buffer_load_b64(rs, off, 0, AUX_SC1);
    if (__all(valid2(f))) break;
    if (spins > SPIN_CAP) { give_up(rs); return false; }
    __builtin_amdgcn_s_sleep(8);
  }
  asm volatile("" ::: "memory");
  return true;
}
__device__ __forceinline__ unsigned wait_pol(__amdgpu_buffer_rsrc_t rs) {
  unsigned v;
  for (unsigned spins = 0;; ++spins) {
    asm volatile("" ::: "memory");
    v = __builtin_amdgcn_raw_buffer_load_b32(rs, XB_POL, 0, AUX_SC1);
    if (v != SENT) break;
    if (spins > SPIN_CAP) { give_up(rs); v = 0; break; }
    __builtin_amdgcn_s_sleep(2);
  }
  return (unsigned)__builtin_amdgcn_readfirstlane((int)v);
}

__device__ __forceinline__ void score_role(unsigned char* __restrict__ ws, __amdgpu_buffer_rsrc_t rs, int h, LAS unsigned char* L) {
  const int tid = threadIdx.x, lane = tid & 63, g = lane >> 4, n = lane & 15;
  const int w = __builtin_amdgcn_readfirstlane(tid >> 6);
  const __amdgpu_buffer_rsrc_t rkx = __builtin_amdgcn_make_buffer_rsrc((void*)(ws + OFF_KX), 0, 64 * 32 * 12 * 1024, 0x00020000);
  v4u wr[48];
  {
    const unsigned char* src = ws + OFF_WF + ((size_t)(h * 24 + 3 * w) * 20) * 1024 + lane * 16;
#pragma unroll
    for (int i = 0; i < 48; ++i) wr[i] = *(const v4u*)(src + i * 1024);
#pragma unroll
    for (int i = 48; i < 60; ++i) *(LAS v4u*)(L + SL_W + (w * 12 + i - 48) * 1024 + lane * 16) = *(const v4u*)(src + i * 1024);
  }
  auto fetch_kx = [&](int seg) -> bool {
    if (!wait_flags2(rs, XB_XFLAG + (seg * 32 + h) * 64)) return false;
    const int src = ((seg * 32 + h) * 12) * 1024 + lane * 16;
    LAS unsigned char* dst = L + SL_KX + (seg & 1) * 12288 + lane * 16;
    const v4u a = __builtin_amdgcn_raw_buffer_load_b128(rkx, src + w * 1024, 0, AUX_SC1);
    const v4u b = __builtin_amdgcn_raw_buffer_load_b128(rkx, src + (8 + (w & 3)) * 1024, 0, AUX_SC1);
    *(LAS v4u*)(dst + w * 1024) = a;
    if (w < 4) *(LAS v4u*)(dst + (8 + w) * 1024) = b;
    return true;
  };
  unsigned pol = 0;
  if (!fetch_kx(0)) return;
  const float qs = w < 4 ? QSCALE : 1.f;
  LAS unsigned char* memrd = L + SL_MEM + (g * 8 + (n & 7)) * 16;
  LAS unsigned char* wl = L + SL_W + (w * 12) * 1024 + lane * 16;
  const int pox = xmap(10240 + h * 640 + (g * 8 + (n & 7)) * 16 + (n >> 3) * 8);
  const int pom = xmap(10240 + h * 640 + 512 + ((g & 1) * 8 + (n & 7)) * 8 + (n >> 3) * 4);
  for (int s = 0; s < NSEG - 1; ++s) {
    if (s == POL_FROM) pol = wait_pol(rs);
    const int soff = slot_off(s, pol);
    STAMP(h, s, 0);
    if (!gather_mem(rs, soff, w, lane, L + SL_MEM)) return;
    STAMP(h, s, 1);
    RAW_BARRIER();
    STAMP(h, s, 2);
    f32x4 acc[3] = {{0, 0, 0, 0}, {0, 0, 0, 0}, {0, 0, 0, 0}};
    {
      v4u bq[3], aq[2];
      bq[0] = ldsr4(memrd); bq[1] = ldsr4(memrd + 512); bq[2] = ldsr4(memrd + 1024);
      SCHED_FENCE();
#pragma unroll
      for (int kk = 0; kk < KS; ++kk) {
        const v4u b = bq[kk % 3];
        v4u a2;
        if (kk < 8) a2 = wr[40 + kk]; else a2 = aq[kk & 1];
        acc[0] = mfma32(wr[kk], b, acc[0]);
        acc[1] = mfma32(wr[20 + kk], b, acc[1]);
        acc[2] = mfma32(a2, b, acc[2]);
        if (kk + 3 < KS) bq[kk % 3] = ldsr4(memrd + (kk + 3) * 512);
        if (kk + 2 >= 8 && kk + 2 < KS) aq[kk & 1] = ldsr4(wl + (kk + 2 - 8) * 1024);
        SCHED_FENCE();
      }
    }
#pragma unroll
    for (int t = 0; t < 3; ++t) {
      const int tau = 3 * w + t;
      v2u o = {pack2(acc[t][0] * qs, acc[t][1] * qs), pack2(acc[t][2] * qs, acc[t][3] * qs)};
      *(LAS v2u*)(L + SL_QK + (tau >> 1) * 1024 + lane * 16 + (tau & 1) * 8) = o;
    }
    STAMP(h, s, 3);
    RAW_BARRIER();
    STAMP(h, s, 4);
    if (w == 0) {
      LAS unsigned char* kx = L + SL_KX + (s & 1) * 12288 + lane * 16;
      LAS unsigned char* qk = L + SL_QK + lane * 16;
      f32x4 sm = {0, 0, 0, 0}, s0 = {0, 0, 0, 0}, s1 = {0, 0, 0, 0};
      v4u fq[2], fm[2], f0[2], f1[2];
      fq[0] = ldsr4(qk); fm[0] = ldsr4(qk + 6 * 1024); f0[0] = ldsr4(kx); f1[0] = ldsr4(kx + 6 * 1024);
      fq[1] = ldsr4(qk + 1024); fm[1] = ldsr4(qk + 7 * 1024); f0[1] = ldsr4(kx + 1024); f1[1] = ldsr4(kx + 7 * 1024);
      SCHED_FENCE();
#pragma unroll
      for (int p = 0; p < 6; ++p) {
        const int b = p & 1;
        sm = mfma32(fm[b], fq[b], sm);
        s0 = mfma32(f0[b], fq[b], s0);
        s1 = mfma32(f1[b], fq[b], s1);
        if (p + 2 < 6) {
          fq[b] = ldsr4(qk + (p + 2) * 1024); fm[b] = ldsr4(qk + (8 + p) * 1024);
          f0[b] = ldsr4(kx + (p + 2) * 1024); f1[b] = ldsr4(kx + (8 + p) * 1024);
        }
        SCHED_FENCE();
      }
      const float NEG = -3.0e38f;
      const bool up = n >= 8;
      float v[6];
      v[0] = up ? s1[0] : s0[0]; v[1] = up ? s1[1] : s0[1]; v[2] = up ? s1[2] : s0[2]; v[3] = up ? s1[3] : s0[3];
      v[4] = up ? sm[2] : sm[0]; v[5] = up ? sm[3] : sm[1];
      if (g >= 2) { v[4] = NEG; v[5] = NEG; }
      float mx = xmax8(max2f(max3f(v[0], v[1], v[2]), max3f(v[3], v[4], v[5])));
      float sum = 0.f;
#pragma unroll
      for (int r = 0; r < 6; ++r) { v[r] = __builtin_amdgcn_exp2f(v[r] - mx); sum += v[r]; }
      const float inv = __builtin_amdgcn_rcpf(xsum8(sum));
      v2u px = {nosent(pack2(v[0] * inv, v[1] * inv)), nosent(pack2(v[2] * inv, v[3] * inv))};
      const unsigned pm = nosent(pack2(v[4] * inv, v[5] * inv));
      __builtin_amdgcn_raw_buffer_store_b64(px, rs, pox, soff, AUX_SC1);
      if (g < 2) __builtin_amdgcn_raw_buffer_store_b32(pm, rs, pom, soff, AUX_SC1);
      STAMP(h, s, 5);
    }
    if (s + 1 < NSEG - 1) { if (!fetch_kx(s + 1)) return; }
  }
}

__device__ __forceinline__ void col_role(unsigned char* __restrict__ ws, unsigned char* __restrict__ xb, __amdgpu_buffer_rsrc_t rs, int c8,
                                         LAS unsigned char* L) {
  const int tid = threadIdx.x, lane = tid & 63, g = lane >> 4, n = lane & 15;
  const int w = __builtin_amdgcn_readfirstlane(tid >> 6);
  const __amdgpu_buffer_rsrc_t rvo = __builtin_amdgcn_make_buffer_rsrc((void*)(ws + OFF_VO), 0, 64 * 80 * 16 * 1280, 0x00020000);
  v4u wr[40];
  {
    const unsigned char* src = ws + OFF_WVF + ((size_t)(c8 * 16 + 2 * w) * 20) * 1024 + lane * 16;
#pragma unroll
    for (int i = 0; i < 40; ++i) wr[i] = *(const v4u*)(src + i * 1024);
  }
  auto stage_x = [&](int seg) {
    const unsigned char* src = ws + OFF_XNF + (size_t)seg * 40960 + w * 5120 + lane * 16;
    LAS unsigned char* dst = L + CL_X + (seg & 1) * 40960 + w * 5120;
#pragma unroll
    for (int i = 0; i < 5; ++i)
      __builtin_amdgcn_global_load_lds((const GAS void*)(src + i * 1024), (LAS void*)(dst + i * 1024), 16, 0, 0);
  };
  stage_x(0);
  stage_x(1);
  __syncthreads();
  v4u vox[2]; v2u vom[2];
  auto compute_vox = [&](int seg) {
    LAS unsigned char* xr = L + CL_X + (seg & 1) * 40960 + lane * 16;
    f32x4 a00 = {0, 0, 0, 0}, a01 = {0, 0, 0, 0}, a10 = {0, 0, 0, 0}, a11 = {0, 0, 0, 0};
    v4u x0[2], x1[2];
    x0[0] = ldsr4(xr); x1[0] = ldsr4(xr + 20 * 1024); x0[1] = ldsr4(xr + 1024); x1[1] = ldsr4(xr + 21 * 1024);
    SCHED_FENCE();
#pragma unroll
    for (int kk = 0; kk < KS; ++kk) {
      const int b = kk & 1;
      a00 = mfma32(x0[b], wr[kk], a00); a01 = mfma32(x1[b], wr[kk], a01);
      a10 = mfma32(x0[b], wr[20 + kk], a10); a11 = mfma32(x1[b], wr[20 + kk], a11);
      if (kk + 2 < KS) { x0[b] = ldsr4(xr + (kk + 2) * 1024); x1[b] = ldsr4(xr + (22 + kk) * 1024); }
      SCHED_FENCE();
    }
    vox[0] = v4u{pack2(a00[0], a00[1]), pack2(a00[2], a00[3]), pack2(a01[0], a01[1]), pack2(a01[2], a01[3])};
    vox[1] = v4u{pack2(a10[0], a10[1]), pack2(a10[2], a10[3]), pack2(a11[0], a11[1]), pack2(a11[2], a11[3])};
  };
  auto store_vo = [&](int seg, const v4u (&vx)[2]) {
    const int dst = ((seg * 80 + c8) * 16 + 2 * w) * 1280;
#pragma unroll
    for (int tt = 0; tt < 2; ++tt) {
      __builtin_amdgcn_raw_buffer_store_b128(vx[tt], rvo, dst + tt * 1280 + lane * 16, 0, AUX_SC1);
      if (g < 2) __builtin_amdgcn_raw_buffer_store_b64(vom[tt], rvo, dst + tt * 1280 + 1024 + lane * 8, 0, AUX_SC1);
    }
  };
  compute_vox(0);
  unsigned pol = 0;
  LAS unsigned char* memrd = L + CL_MEM + (g * 8 + (n & 7)) * 16;
  const int oa = xmap(10240 + (4 * w + (n >> 3)) * 640 + (g * 8 + (n & 7)) * 16);
  const int ob = xmap(10240 + (4 * w + 2 + (n >> 3)) * 640 + (g * 8 + (n & 7)) * 16);
  const int om = xmap(10240 + (4 * w + 2 * (g >> 1) + (n >> 3)) * 640 + 512 + ((g & 1) * 8 + (n & 7)) * 8);
  const int opub = xmap(c8 * 128 + n * 8);
  for (int s = 0; s < NSEG; ++s) {
    if (s == POL_FROM - 1) pol = wait_pol(rs);
    const int soff = slot_off(s, pol);
    STAMP(32 + c8, s, 0);
    if (!gather_mem(rs, soff, w, lane, L + CL_MEM)) return;
    __syncthreads();
    STAMP(32 + c8, s, 1);
    if (s >= 1 && tid == 128) __hip_atomic_fetch_add((GAS unsigned*)(xb + XB_VOCNT + (s - 1) * 128), 1u, __ATOMIC_RELAXED, __HIP_MEMORY_SCOPE_AGENT);
    if (s >= 1 && s + 1 < NSEG) stage_x(s + 1);
    if (c8 == 0 && tid == 64) __builtin_amdgcn_raw_buffer_store_b32((unsigned)(s + 1), rs, XB_PROG, 0, AUX_SC1);
    {
      f32x4 m0 = {0, 0, 0, 0}, m1 = {0, 0, 0, 0};
      v4u aq[3];
      aq[0] = ldsr4(memrd); aq[1] = ldsr4(memrd + 512); aq[2] = ldsr4(memrd + 1024);
      SCHED_FENCE();
#pragma unroll
      for (int kk = 0; kk < KS; ++kk) {
        const v4u a = aq[kk % 3];
        m0 = mfma32(a, wr[kk], m0); m1 = mfma32(a, wr[20 + kk], m1);
        if (kk + 3 < KS) aq[kk % 3] = ldsr4(memrd + (kk + 3) * 512);
        SCHED_FENCE();
      }
      vom[0] = v2u{pack2(m0[0], m0[1]), pack2(m0[2], m0[3])};
      vom[1] = v2u{pack2(m1[0], m1[1]), pack2(m1[2], m1[3])};
    }
    if (s == NSEG - 1) {
      store_vo(s, vox);
      asm volatile("s_waitcnt vmcnt(0)" ::: "memory");
      RAW_BARRIER();
      if (tid == 64) __hip_atomic_fetch_add((GAS unsigned*)(xb + XB_VOCNT + s * 128), 1u, __ATOMIC_RELAXED, __HIP_MEMORY_SCOPE_AGENT);
      break;
    }
    STAMP(32 + c8, s, 2);
    v4u pa, pb; v2u pmv;
    for (unsigned spins = 0;; ++spins) {
      asm volatile("" ::: "memory");
      pa = __builtin_amdgcn_raw_buffer_load_b128(rs, oa, soff, AUX_SC1);
      pb = __builtin_amdgcn_raw_buffer_load_b128(rs, ob, soff, AUX_SC1);
      pmv = __builtin_amdgcn_raw_buffer_load_b64(rs, om, soff, AUX_SC1);
      if (__all(valid4(pa) & valid4(pb) & valid2(pmv))) break;
      if (spins > SPIN_CAP) { give_up(rs); return; }
    }
    STAMP(32 + c8, s, 3);
    {
      const v2u z = {0u, 0u};
      const v2u pm0 = g < 2 ? pmv : z, pm1 = g < 2 ? z : pmv;
      f32x4 d0 = {0, 0, 0, 0}, d1 = {0, 0, 0, 0}, d2 = {0, 0, 0, 0};
      d0 = mfma32(vox[0], pa, d0); d1 = mfma32(vox[1], pb, d1);
      d2 = mfma32(v4u{vom[0].x, vom[0].y, vom[1].x, vom[1].y}, v4u{pm0.x, pm0.y, pm1.x, pm1.y}, d2);
      d0 += d1 + d2;
      if ((g >> 1) == (n >> 3)) *(LAS f32x4*)(L + CL_PART + w * 512 + (n >> 3) * 256 + (n & 7) * 32 + (g & 1) * 16) = d0;
    }
    RAW_BARRIER();
    STAMP(32 + c8, s, 4);
    if (w == 0) {
      LAS unsigned char* pr = L + CL_PART + g * 1024 + n * 16;
      f32x4 t = (*(LAS f32x4*)(pr) + *(LAS f32x4*)(pr + 256)) + (*(LAS f32x4*)(pr + 512) + *(LAS f32x4*)(pr + 768));
      t[0] = xsum4(t[0]); t[1] = xsum4(t[1]); t[2] = xsum4(t[2]); t[3] = xsum4(t[3]);
      v2u o = {nosent(pack2(t[0], t[1])), nosent(pack2(t[2], t[3]))};
      if (g == 0) __builtin_amdgcn_raw_buffer_store_b64(o, rs, opub, slot_off(s + 1, pol), AUX_SC1);
    }
    STAMP(32 + c8, s, 5);
    SCHED_FENCE();
    RAW_BARRIER();
    const v4u vold[2] = {vox[0], vox[1]};
    compute_vox(s + 1);
    SCHED_FENCE();
    store_vo(s, vold);
    STAMP(32 + c8, s, 6);
  }
}

__device__ __forceinline__ void lds_sync6(volatile LAS unsigned* cnt, unsigned target, int lane) {
  asm volatile("s_waitcnt lgkmcnt(0)" ::: "memory");
  if (lane == 0) __hip_atomic_fetch_add((LAS unsigned*)cnt, 1u, __ATOMIC_RELAXED, __HIP_MEMORY_SCOPE_WORKGROUP);
  while (*cnt < target) __builtin_amdgcn_s_sleep(1);
  asm volatile("" ::: "memory");
}

__device__ __forceinline__ void xs_role(unsigned char* __restrict__ ws, unsigned char* __restrict__ xb, __amdgpu_buffer_rsrc_t rs, int h, int sp,
                                        LAS unsigned char* L) {
  const int tid = threadIdx.x, lane = tid & 63, g = lane >> 4, n = lane & 15;
  const int w = __builtin_amdgcn_readfirstlane(tid >> 6);
  const __amdgpu_buffer_rsrc_t rq = __builtin_amdgcn_make_buffer_rsrc((void*)(ws + OFF_QX), 0, (int)(OFF_VO - OFF_QX), 0x00020000);
  constexpr int RO_KX = (int)(OFF_KX - OFF_QX), RO_KM = (int)(OFF_KM - OFF_QX), RO_PX = (int)(OFF_PX - OFF_QX);
  volatile LAS unsigned* cnt = (volatile LAS unsigned*)(L + XL_CNT);
  const bool isk = w < 3;
  const int pl = isk ? w : (w == 3 ? 2 : (w - 4) & 1);
  v4u wr[40];
  if (w < 6) {
    const unsigned char* src = ws + OFF_WF + ((size_t)(h * 24 + (isk ? 12 : 0) + 6 * sp + 2 * pl) * 20) * 1024 + lane * 16;
#pragma unroll
    for (int i = 0; i < 40; ++i) wr[i] = *(const v4u*)(src + i * 1024);
  } else {
#pragma unroll
    for (int i = 0; i < 40; ++i) wr[i] = v4u{0u, 0u, 0u, 0u};
  }
  if (tid == 0) cnt[0] = 0u;
  auto stage_x = [&](int seg) {
    const unsigned char* src = ws + OFF_XNF + (size_t)seg * 40960 + (w - 6) * 20480 + lane * 16;
    LAS unsigned char* dst = L + XL_X + (seg & 1) * 40960 + (w - 6) * 20480;
#pragma unroll
    for (int i = 0; i < 20; ++i)
      __builtin_amdgcn_global_load_lds((const GAS void*)(src + i * 1024), (LAS void*)(dst + i * 1024), 16, 0, 0);
  };
  if (w >= 6) stage_x(0);
  asm volatile("s_waitcnt vmcnt(0)" ::: "memory");
  const float sc = isk ? 1.f : QSCALE;
  for (int s = 0; s < NSEG + 2; ++s) {
    RAW_BARRIER();
    if (s >= 2 && tid == 448) __builtin_amdgcn_raw_buffer_store_b32(1u, rs, XB_XFLAG + ((s - 2) * 32 + h) * 64 + sp * 4, 0, AUX_SC1);
    if (w >= 6) {
      if (s + 1 < NSEG) stage_x(s + 1);
      asm volatile("s_waitcnt vmcnt(0)" ::: "memory");
    } else if (s < NSEG) {
      LAS unsigned char* xr = L + XL_X + (s & 1) * 40960 + lane * 16;
      f32x4 a00 = {0, 0, 0, 0}, a01 = {0, 0, 0, 0}, a10 = {0, 0, 0, 0}, a11 = {0, 0, 0, 0};
      v4u x0[2], x1[2];
      x0[0] = ldsr4(xr); x1[0] = ldsr4(xr + 20 * 1024); x0[1] = ldsr4(xr + 1024); x1[1] = ldsr4(xr + 21 * 1024);
      SCHED_FENCE();
#pragma unroll
      for (int kk = 0; kk < KS; ++kk) {
        const int b = kk & 1;
        a00 = mfma32(wr[kk], x0[b], a00); a01 = mfma32(wr[kk], x1[b], a01);
        a10 = mfma32(wr[20 + kk], x0[b], a10); a11 = mfma32(wr[20 + kk], x1[b], a11);
        if (kk + 2 < KS) { x0[b] = ldsr4(xr + (kk + 2) * 1024); x1[b] = ldsr4(xr + (22 + kk) * 1024); }
        SCHED_FENCE();
      }
      const v4u o0 = {pack2(a00[0] * sc, a00[1] * sc), pack2(a00[2] * sc, a00[3] * sc), pack2(a10[0] * sc, a10[1] * sc), pack2(a10[2] * sc, a10[3] * sc)};
      const v4u o1 = {pack2(a01[0] * sc, a01[1] * sc), pack2(a01[2] * sc, a01[3] * sc), pack2(a11[0] * sc, a11[1] * sc), pack2(a11[2] * sc, a11[3] * sc)};
      const int dst = (isk ? RO_KX : 0) + ((s * 32 + h) * 12 + 3 * sp + pl) * 1024 + lane * 16;
      __builtin_amdgcn_raw_buffer_store_b128(o0, rq, dst, 0, AUX_SC1);
      __builtin_amdgcn_raw_buffer_store_b128(o1, rq, dst + 6 * 1024, 0, AUX_SC1);
      asm volatile("s_waitcnt vmcnt(2)" ::: "memory");
    } else {
      asm volatile("s_waitcnt vmcnt(0)" ::: "memory");
    }
  }
  const unsigned pol = wait_pol(rs);
  if (w < 6) {
    LAS unsigned char* memrd = L + XL_MEM + (g * 8 + (n & 7)) * 16;
    const int p0 = 128 * w + lane, o0 = xmap(p0 * 16), o1 = xmap(p0 * 16 + 1024);
    for (int s = 0; s < NSEG; ++s) {
      if (w < 5) {
        for (unsigned spins = 0;; ++spins) {
          asm volatile("" ::: "memory");
          const unsigned pr = __builtin_amdgcn_raw_buffer_load_b32(rs, XB_PROG, 0, AUX_SC1);
          if (pr != SENT && pr > (unsigned)s) break;
          if (spins > SPIN_CAP) { give_up(rs); break; }
          __builtin_amdgcn_s_sleep(16);
        }
        const int soff = slot_off(s, pol);
        v4u v0, v1;
        for (unsigned spins = 0;; ++spins) {
          asm volatile("" ::: "memory");
          v0 = __builtin_amdgcn_raw_buffer_load_b128(rs, o0, soff, AUX_SC1);
          v1 = __builtin_amdgcn_raw_buffer_load_b128(rs, o1, soff, AUX_SC1);
          if (__all(valid4(v0) & valid4(v1))) break;
          if (spins > SPIN_CAP) { give_up(rs); break; }
          __builtin_amdgcn_s_sleep(8);
        }
        *(LAS v4u*)(L + XL_MEM + p0 * 16) = v0;
        *(LAS v4u*)(L + XL_MEM + p0 * 16 + 1024) = v1;
      }
      lds_sync6(cnt, (unsigned)(12 * s + 6), lane);
      if (isk) {
        f32x4 m0 = {0, 0, 0, 0}, m1 = {0, 0, 0, 0};
        v4u aq[3];
        aq[0] = ldsr4(memrd); aq[1] = ldsr4(memrd + 512); aq[2] = ldsr4(memrd + 1024);
        SCHED_FENCE();
#pragma unroll
        for (int kk = 0; kk < KS; ++kk) {
          const v4u b = aq[kk % 3];
          m0 = mfma32(wr[kk], b, m0); m1 = mfma32(wr[20 + kk], b, m1);
          if (kk + 3 < KS) aq[kk % 3] = ldsr4(memrd + (kk + 3) * 512);
          SCHED_FENCE();
        }
        const v4u o = {pack2(m0[0], m0[1]), pack2(m0[2], m0[3]), pack2(m1[0], m1[1]), pack2(m1[2], m1[3])};
        __builtin_amdgcn_raw_buffer_store_b128(o, rq, RO_KM + ((s * 32 + h) * 6 + 3 * sp + pl) * 1024 + lane * 16, 0, AUX_SC1);
        asm volatile("s_waitcnt vmcnt(0)" ::: "memory");
      }
      lds_sync6(cnt, (unsigned)(12 * s + 12), lane);
      if (tid == 0) __builtin_amdgcn_raw_buffer_store_b32(1u, rs, XB_KMFLAG + (s * 32 + h) * 64 + sp * 4, 0, AUX_SC1);
    }
  } else {
    const int rt = sp;
    for (int s = (w & 1); s < NSEG; s += 2) {
      if (!wait_flags2(rs, XB_XFLAG + (s * 32 + h) * 64)) return;
      const int qo = ((s * 32 + h) * 12 + rt * 6) * 1024 + lane * 16;
      const int ko = RO_KX + ((s * 32 + h) * 12) * 1024 + lane * 16;
      v4u fq[6], f0[6], f1[6], fm[6];
#pragma unroll
      for (int p = 0; p < 6; ++p) {
        fq[p] = __builtin_amdgcn_raw_buffer_load_b128(rq, qo + p * 1024, 0, AUX_SC1);
        f0[p] = __builtin_amdgcn_raw_buffer_load_b128(rq, ko + p * 1024, 0, AUX_SC1);
        f1[p] = __builtin_amdgcn_raw_buffer_load_b128(rq, ko + (6 + p) * 1024, 0, AUX_SC1);
      }
      f32x4 sm = {0, 0, 0, 0}, s0 = {0, 0, 0, 0}, s1 = {0, 0, 0, 0};
#pragma unroll
      for (int p = 0; p < 6; ++p) { s0 = mfma32(f0[p], fq[p], s0); s1 = mfma32(f1[p], fq[p], s1); }
      {
        const unsigned need = (unsigned)(12 * (s + 1 < NSEG ? s + 1 : s) + 12);
        unsigned spins = 0;
        while (*cnt < need) { __builtin_amdgcn_s_sleep(8); if (++spins > SPIN_CAP) break; }
      }
      if (!wait_flags2(rs, XB_KMFLAG + (s * 32 + h) * 64)) return;
      const int mo = RO_KM + ((s * 32 + h) * 6) * 1024 + lane * 16;
#pragma unroll
      for (int p = 0; p < 6; ++p) fm[p] = __builtin_amdgcn_raw_buffer_load_b128(rq, mo + p * 1024, 0, AUX_SC1);
#pragma unroll
      for (int p = 0; p < 6; ++p) sm = mfma32(fm[p], fq[p], sm);
      const float NEG = -3.0e38f;
      const int t = rt * 16 + n;
      if (g >= 2) sm = f32x4{NEG, NEG, NEG, NEG};
#pragma unroll
      for (int r = 0; r < 4; ++r) {
        if (4 * g + r > t) s0[r] = NEG;
        if (16 + 4 * g + r > t) s1[r] = NEG;
      }
      float mx = max3f(max3f(sm[0], sm[1], sm[2]), max3f(sm[3], s0[0], s0[1]), max3f(max3f(s0[2], s0[3], s1[0]), s1[1], max2f(s1[2], s1[3])));
      mx = xmax4(mx);
      float em[4], e0[4], e1[4], sum = 0.f;
#pragma unroll
      for (int r = 0; r < 4; ++r) {
        em[r] = __builtin_amdgcn_exp2f(sm[r] - mx); e0[r] = __builtin_amdgcn_exp2f(s0[r] - mx); e1[r] = __builtin_amdgcn_exp2f(s1[r] - mx);
        sum += em[r] + e0[r] + e1[r];
      }
      const float inv = 1.f / xsum4(sum);
      const v4u px = {pack2(e0[0] * inv, e0[1] * inv), pack2(e0[2] * inv, e0[3] * inv), pack2(e1[0] * inv, e1[1] * inv), pack2(e1[2] * inv, e1[3] * inv)};
      const v2u pm = {pack2(em[0] * inv, em[1] * inv), pack2(em[2] * inv, em[3] * inv)};
      const int rg = 2 * rt + (n >> 3), l2 = g * 16 + (h & 1) * 8 + (n & 7);
      const int dst = RO_PX + ((s * 16 + (h >> 1)) * 4 + rg) * 1536;
      __builtin_amdgcn_raw_buffer_store_b128(px, rq, dst + l2 * 16, 0, AUX_SC1);
      __builtin_amdgcn_raw_buffer_store_b64(pm, rq, dst + 1024 + l2 * 8, 0, AUX_SC1);
      asm volatile("s_waitcnt vmcnt(0)" ::: "memory");
      if (lane == 0) __hip_atomic_fetch_add((GAS unsigned*)(xb + XB_PXCNT + s * 128), 1u, __ATOMIC_RELAXED, __HIP_MEMORY_SCOPE_AGENT);
    }
  }
}

__device__ __forceinline__ void ao_role(unsigned char* __restrict__ ws, __amdgpu_buffer_rsrc_t rs, int j, LAS unsigned char* L) {
  const int tid = threadIdx.x, lane = tid & 63, g = lane >> 4, n = lane & 15;
  const int w = __builtin_amdgcn_readfirstlane(tid >> 6);
  float* hout = (float*)(ws + OFF_H);
  for (int u = j; u < NSEG * 10; u += NAO) {
    const int s = u / 10, f = u % 10;
    if (w == 0) {
      for (unsigned spins = 0;; ++spins) {
        asm volatile("" ::: "memory");
        const unsigned a = __builtin_amdgcn_raw_buffer_load_b32(rs, XB_VOCNT + s * 128, 0, AUX_SC1);
        const unsigned b = __builtin_amdgcn_raw_buffer_load_b32(rs, XB_PXCNT + s * 128, 0, AUX_SC1);
        if (a == (unsigned)(NCOL - 1) && b == 63u) break;
        if (spins > SPIN_CAP) { give_up(rs); break; }
        __builtin_amdgcn_s_sleep(16);
      }
      __builtin_amdgcn_fence(__ATOMIC_ACQUIRE, "agent");
      asm volatile("s_waitcnt vmcnt(0)" ::: "memory");
    }
    __syncthreads();
    {
      const unsigned char* src = ws + OFF_PX + (size_t)s * 98304 + w * 12288 + lane * 16;
#pragma unroll
      for (int i = 0; i < 12; ++i)
        __builtin_amdgcn_global_load_lds((const GAS void*)(src + i * 1024), (LAS void*)(L + w * 12288 + i * 1024), 16, 0, 0);
    }
    const int c0 = f * 8 + w;
    const unsigned char* vsrc = ws + OFF_VO + ((size_t)(s * 80 + c0) * 16) * 1280;
    v4u vx[16]; v2u vm[16];
#pragma unroll
    for (int k = 0; k < 16; ++k) {
      vx[k] = *(const v4u*)(vsrc + k * 1280 + lane * 16);
      vm[k] = *(const v2u*)(vsrc + k * 1280 + 1024 + (lane & 31) * 8);
    }
    SCHED_FENCE();
    __syncthreads();
    f32x4 acc[4] = {{0, 0, 0, 0}, {0, 0, 0, 0}, {0, 0, 0, 0}, {0, 0, 0, 0}};
#pragma unroll
    for (int hq = 0; hq < 8; ++hq) {
      const v4u vmm = {vm[2 * hq].x, vm[2 * hq].y, vm[2 * hq + 1].x, vm[2 * hq + 1].y};
#pragma unroll
      for (int rg = 0; rg < 4; ++rg) {
        const v4u px0 = *(LAS v4u*)(L + (hq * 8 + rg) * 1536 + lane * 16);
        const v4u px1 = *(LAS v4u*)(L + (hq * 8 + 4 + rg) * 1536 + lane * 16);
        const v2u pm0 = *(LAS v2u*)(L + (hq * 8 + rg) * 1536 + 1024 + lane * 8);
        const v2u pm1 = *(LAS v2u*)(L + (hq * 8 + 4 + rg) * 1536 + 1024 + lane * 8);
        acc[rg] = mfma32(vx[2 * hq], px0, acc[rg]);
        acc[rg] = mfma32(vx[2 * hq + 1], px1, acc[rg]);
        acc[rg] = mfma32(vmm, v4u{pm0.x, pm0.y, pm1.x, pm1.y}, acc[rg]);
      }
    }
#pragma unroll
    for (int rg = 0; rg < 4; ++rg) {
      f32x4 o;
#pragma unroll
      for (int r = 0; r < 4; ++r) o[r] = acc[rg][r] + __shfl(acc[rg][r], (lane + 40) & 63);
      if (g < 2 && n < 8) *(f32x4*)(hout + (size_t)(s * 32 + rg * 8 + n) * DM + c0 * 8 + g * 4) = o;
    }
  }
}

__global__ void __launch_bounds__(CH_T, 2) k_chain(unsigned char* __restrict__ ws, unsigned char* __restrict__ xb) {
  extern __shared__ __attribute__((aligned(16))) unsigned char smem[];
  LAS unsigned char* L = (LAS unsigned char*)smem;
  const __amdgpu_buffer_rsrc_t rs = __builtin_amdgcn_make_buffer_rsrc((void*)xb, 0, XB_SIZE, 0x00020000);
  if (threadIdx.x == 0) {
    GAS unsigned* cA = (GAS unsigned*)(xb + XB_CNTA); GAS unsigned* cB = (GAS unsigned*)(xb + XB_CNTB);
    GAS unsigned* cAll = (GAS unsigned*)(xb + XB_CNTALL); GAS unsigned* cF = (GAS unsigned*)(xb + XB_CNTF);
    unsigned xcc; asm volatile("s_getreg_b32 %0, hwreg(HW_REG_XCC_ID)" : "=s"(xcc));
    int role = -1;
    if ((xcc & 0xf) < 4) {
      const unsigned t = __hip_atomic_fetch_add(cA, 1u, __ATOMIC_RELAXED, __HIP_MEMORY_SCOPE_AGENT) + 1u;
      if (t <= (unsigned)NCH) role = (int)t;
    }
    if (role < 0) {
      const unsigned t = __hip_atomic_fetch_add(cB, 1u, __ATOMIC_RELAXED, __HIP_MEMORY_SCOPE_AGENT) + 1u;
      if (t < (unsigned)(NXS + NAO)) role = NCH + 1 + (int)t;
    }
    asm volatile("s_waitcnt vmcnt(0)" ::: "memory");
    __hip_atomic_fetch_add(cAll, 1u, __ATOMIC_RELAXED, __HIP_MEMORY_SCOPE_AGENT);
    if (role < 0) {
      for (unsigned spins = 0; spins < SPIN_CAP; ++spins) {
        const unsigned na = __hip_atomic_load(cA, __ATOMIC_RELAXED, __HIP_MEMORY_SCOPE_AGENT) + 1u;
        if (na > (unsigned)NCH) break;
        const unsigned nall = __hip_atomic_load(cAll, __ATOMIC_RELAXED, __HIP_MEMORY_SCOPE_AGENT) + 1u;
        if (nall >= (unsigned)CH_GRID) {
          const unsigned na2 = __hip_atomic_load(cA, __ATOMIC_RELAXED, __HIP_MEMORY_SCOPE_AGENT) + 1u;
          if (na2 <= (unsigned)NCH) {
            const unsigned t = __hip_atomic_fetch_add(cF, 1u, __ATOMIC_RELAXED, __HIP_MEMORY_SCOPE_AGENT) + 1u;
            if (na2 + t <= (unsigned)NCH) role = (int)(na2 + t);
          }
          break;
        }
        __builtin_amdgcn_s_sleep(8);
      }
    }
    *(LAS int*)(L + SL_ROLE) = role;
  }
  __syncthreads();
  const int role = __builtin_amdgcn_readfirstlane(*(LAS int*)(L + SL_ROLE));
  if (role < 0) return;
  if (role < NCH) {
    const int grp = role / 7, r = role % 7;
    if (r < 2) score_role(ws, rs, grp * 2 + r, L);
    else col_role(ws, xb, rs, grp * 5 + r - 2, L);
  } else if (role == NCH) {
    if (threadIdx.x < 64) {
      int best[4] = {0x7fffffff, 0x7fffffff, 0x7fffffff, 0x7fffffff};
      for (int it = 0; it < 7; ++it) {
#pragma unroll
        for (int c = 0; c < 4; ++c) {
          const int off = (c >> 1) * 32 * 65536 + xmap(31 * 1024) + (c & 1) * 8192;
          const long long t0 = __builtin_amdgcn_s_memtime();
          __builtin_amdgcn_raw_buffer_store_b32((unsigned)it, rs, off, 0, AUX_SC1);
          asm volatile("s_waitcnt vmcnt(0)" ::: "memory");
          unsigned v = __builtin_amdgcn_raw_buffer_load_b32(rs, off, 0, AUX_SC1);
          asm volatile("s_waitcnt vmcnt(0)" : "+v"(v) :: "memory");
          const int dt = (int)(__builtin_amdgcn_s_memtime() - t0);
          best[c] = dt < best[c] ? dt : best[c];
        }
      }
      const unsigned pv = (best[1] < best[0] ? 1u : 0u) | (best[3] < best[2] ? 2u : 0u);
      if (threadIdx.x == 0) __builtin_amdgcn_raw_buffer_store_b32(pv, rs, XB_POL, 0, AUX_SC1);
    }
  } else {
    if (role < NCH + 1 + NXS) xs_role(ws, xb, rs, (role - NCH - 1) >> 1, (role - NCH - 1) & 1, L);
    else ao_role(ws, rs, role - (NCH + 1 + NXS), L);
  }
}

__device__ __forceinline__ float gelu_tanh(float x) {
  const float u = 0.7978845608028654f * (x + 0.044715f * x * x * x);
  const float e = __expf(2.f * u);
  const float th = 1.f - 2.f / (e + 1.f);
  return 0.5f * x * (1.f + th);
}
__global__ void __launch_bounds__(512) k_ffn1(const u16* __restrict__ x, unsigned char* __restrict__ ws) {
  extern __shared__ __attribute__((aligned(16))) unsigned char smem[];
  LAS unsigned char* L = (LAS unsigned char*)smem;
  const int tid = threadIdx.x, lane = tid & 63, w = __builtin_amdgcn_readfirstlane(tid >> 6);
  const int rb = blockIdx.x & 31, nbk = blockIdx.x >> 5;
  const int fp = nbk * 8 + w;
  const unsigned char* wsrc = ws + OFF_WGU + ((size_t)(fp * 2) * 2 * 20) * 1024 + lane * 16;
  v4u wq[3][4];
#pragma unroll
  for (int k = 0; k < 3; ++k)
#pragma unroll
    for (int i = 0; i < 4; ++i) wq[k][i] = *(const v4u*)(wsrc + (i * 20 + k) * 1024);
  SCHED_FENCE();
#pragma unroll
  for (int ps = 0; ps < 2; ++ps) {
    const int rl = w * 8 + ps * 4 + (lane >> 4), q = lane & 15;
    const float* hsrc = (const float*)(ws + OFF_H) + (size_t)(rb * 64 + rl) * DM;
    float v[5][8];
    float s = 0.f;
#pragma unroll
    for (int i = 0; i < 5; ++i) {
      f32x4 a = *(const f32x4*)(hsrc + (q + 16 * i) * 8), b = *(const f32x4*)(hsrc + (q + 16 * i) * 8 + 4);
      const v4u xv = *(const v4u*)(x + (size_t)(rb * 64 + rl) * DM + (q + 16 * i) * 8);
      a[0] += bflo(xv.x); a[1] += bfhi(xv.x); a[2] += bflo(xv.y); a[3] += bfhi(xv.y);
      b[0] += bflo(xv.z); b[1] += bfhi(xv.z); b[2] += bflo(xv.w); b[3] += bfhi(xv.w);
#pragma unroll
      for (int j = 0; j < 4; ++j) { v[i][j] = a[j]; v[i][4 + j] = b[j]; s += a[j] + b[j]; }
    }
#pragma unroll
    for (int o = 1; o < 16; o <<= 1) s += __shfl_xor(s, o);
    const float mean = s * (1.f / DM);
    float qq = 0.f;
#pragma unroll
    for (int i = 0; i < 5; ++i)
#pragma unroll
      for (int j = 0; j < 8; ++j) { float d = v[i][j] - mean; qq += d * d; }
#pragma unroll
    for (int o = 1; o < 16; o <<= 1) qq += __shfl_xor(qq, o);
    const float rs = 1.f / sqrtf(qq * (1.f / DM) + 1e-5f);
#pragma unroll
    for (int i = 0; i < 5; ++i) {
      const int pc = q + 16 * i, kk = pc >> 2, g = pc & 3;
      v4u o;
      o.x = pack2((v[i][0] - mean) * rs, (v[i][1] - mean) * rs);
      o.y = pack2((v[i][2] - mean) * rs, (v[i][3] - mean) * rs);
      o.z = pack2((v[i][4] - mean) * rs, (v[i][5] - mean) * rs);
      o.w = pack2((v[i][6] - mean) * rs, (v[i][7] - mean) * rs);
      *(LAS v4u*)(L + ((rl >> 4) * 20 + kk) * 1024 + (g * 16 + (rl & 15)) * 16) = o;
    }
  }
  __syncthreads();
  f32x4 acc[4][4];
#pragma unroll
  for (int i = 0; i < 4; ++i)
#pragma unroll
    for (int m = 0; m < 4; ++m) acc[i][m] = f32x4{0, 0, 0, 0};
  LAS unsigned char* xr = L + lane * 16;
  v4u bq[2][4];
#pragma unroll
  for (int m = 0; m < 4; ++m) bq[0][m] = *(LAS v4u*)(xr + (m * 20) * 1024);
#pragma unroll
  for (int kk = 0; kk < KS; ++kk) {
    if (kk + 1 < KS) {
#pragma unroll
      for (int m = 0; m < 4; ++m) bq[(kk + 1) & 1][m] = *(LAS v4u*)(xr + (m * 20 + kk + 1) * 1024);
    }
    SCHED_FENCE();
#pragma unroll
    for (int i = 0; i < 4; ++i)
#pragma unroll
      for (int m = 0; m < 4; ++m) acc[i][m] = mfma32(wq[kk % 3][i], bq[kk & 1][m], acc[i][m]);
    SCHED_FENCE();
    if (kk + 3 < KS) {
#pragma unroll
      for (int i = 0; i < 4; ++i) wq[kk % 3][i] = *(const v4u*)(wsrc + (i * 20 + kk + 3) * 1024);
    }
    SCHED_FENCE();
  }
#pragma unroll
  for (int m = 0; m < 4; ++m) {
    float o[8];
#pragma unroll
    for (int r = 0; r < 4; ++r) {
      o[r] = gelu_tanh(acc[0][m][r]) * acc[1][m][r];
      o[4 + r] = gelu_tanh(acc[2][m][r]) * acc[3][m][r];
    }
    v4u ov = {pack2(o[0], o[1]), pack2(o[2], o[3]), pack2(o[4], o[5]), pack2(o[6], o[7])};
    *(v4u*)(ws + OFF_AF + ((size_t)(rb * 4 + m) * 48 + fp) * 1024 + lane * 16) = ov;
  }
}

__global__ void __launch_bounds__(512) k_ffn2(const u16* __restrict__ x, unsigned char* __restrict__ ws, u16* __restrict__ out) {
  extern __shared__ __attribute__((aligned(16))) unsigned char smem[];
  LAS unsigned char* L = (LAS unsigned char*)smem;
  const int tid = threadIdx.x, lane = tid & 63, g = lane >> 4, n = lane & 15, w = __builtin_amdgcn_readfirstlane(tid >> 6);
  const int rb = blockIdx.x & 31, cb = blockIdx.x >> 5;
  const unsigned char* wsrc = ws + OFF_WD + ((size_t)(cb * 5) * 48 + 6 * w) * 1024 + lane * 16;
  const unsigned char* xsrc = ws + OFF_AF + ((size_t)(rb * 4) * 48 + 6 * w) * 1024 + lane * 16;
  f32x4 acc[5][4];
#pragma unroll
  for (int i = 0; i < 5; ++i)
#pragma unroll
    for (int m = 0; m < 4; ++m) acc[i][m] = f32x4{0, 0, 0, 0};
  v4u a[3][5], b[3][4];
#pragma unroll
  for (int k = 0; k < 3; ++k) {
#pragma unroll
    for (int i = 0; i < 5; ++i) a[k][i] = *(const v4u*)(wsrc + (i * 48 + k) * 1024);
#pragma unroll
    for (int m = 0; m < 4; ++m) b[k][m] = *(const v4u*)(xsrc + (m * 48 + k) * 1024);
  }
  SCHED_FENCE();
#pragma unroll
  for (int k = 0; k < 6; ++k) {
#pragma unroll
    for (int i = 0; i < 5; ++i)
#pragma unroll
      for (int m = 0; m < 4; ++m) acc[i][m] = mfma32(a[k % 3][i], b[k % 3][m], acc[i][m]);
    SCHED_FENCE();
    if (k + 3 < 6) {
#pragma unroll
      for (int i = 0; i < 5; ++i) a[k % 3][i] = *(const v4u*)(wsrc + (i * 48 + k + 3) * 1024);
#pragma unroll
      for (int m = 0; m < 4; ++m) b[k % 3][m] = *(const v4u*)(xsrc + (m * 48 + k + 3) * 1024);
    }
    SCHED_FENCE();
  }
#pragma unroll
  for (int i = 0; i < 5; ++i)
#pragma unroll
    for (int m = 0; m < 4; ++m) *(LAS f32x4*)(L + (w * 20 + i * 4 + m) * 1024 + lane * 16) = acc[i][m];
  __syncthreads();
  const float* hsrc = (const float*)(ws + OFF_H);
  for (int tl = w; tl < 20; tl += 8) {
    f32x4 s = *(LAS f32x4*)(L + tl * 1024 + lane * 16);
#pragma unroll
    for (int ww = 1; ww < 8; ++ww) s += *(LAS f32x4*)(L + (ww * 20 + tl) * 1024 + lane * 16);
    const int i = tl >> 2, m = tl & 3;
    const int t = (rb * 4 + m) * 16 + n, col = (cb * 5 + i) * 16 + g * 4;
    f32x4 hv = *(const f32x4*)(hsrc + (size_t)t * DM + col);
    const v2u xv = *(const v2u*)(x + (size_t)t * DM + col);
    hv[0] += bflo(xv.x); hv[1] += bfhi(xv.x); hv[2] += bflo(xv.y); hv[3] += bfhi(xv.y);
    v2u o = {pack2(s[0] + hv[0], s[1] + hv[1]), pack2(s[2] + hv[2], s[3] + hv[3])};
    *(v2u*)(out + (size_t)t * DM + col) = o;
  }
}

static void launch_front(void* const* d_in, void* d_ws, hipStream_t st) {
  const u16* x = (const u16*)d_in[0]; const u16* mem0 = (const u16*)d_in[1];
  const u16* Wq = (const u16*)d_in[2]; const u16* Wk = (const u16*)d_in[3]; const u16* Wv = (const u16*)d_in[4]; const u16* Wo = (const u16*)d_in[5];
  const u16* wg = (const u16*)d_in[8]; const u16* wu = (const u16*)d_in[9]; const u16* wd = (const u16*)d_in[10];
  unsigned char* ws = (unsigned char*)d_ws;
  unsigned char* xb = (unsigned char*)(((uintptr_t)ws + (2u << 20) - 1) & ~(uintptr_t)((2u << 20) - 1));
  (void)hipFuncSetAttribute((const void*)k_chain, hipFuncAttributeMaxDynamicSharedMemorySize, CH_LDS);
  k_pre<<<PRE_GRID, 256, 0, st>>>(x, mem0, Wq, Wk, Wv, Wo, wg, wu, wd, ws, xb);
  k_chain<<<CH_GRID, CH_T, CH_LDS, st>>>(ws, xb);
}
static void run(void* const* d_in, void* d_out, void* d_ws, hipStream_t st) {
  const u16* x = (const u16*)d_in[0];
  unsigned char* ws = (unsigned char*)d_ws;
  (void)hipFuncSetAttribute((const void*)k_ffn1, hipFuncAttributeMaxDynamicSharedMemorySize, 81920);
  (void)hipFuncSetAttribute((const void*)k_ffn2, hipFuncAttributeMaxDynamicSharedMemorySize, 163840);
  launch_front(d_in, d_ws, st);
  k_ffn1<<<32 * 6, 512, 81920, st>>>(x, ws);
  k_ffn2<<<256, 512, 163840, st>>>(x, ws, (u16*)d_out);
}
}

extern "C" void kernel_launch(void* const* d_in, const int* in_sizes, int n_in, void* d_out, int out_size, void* d_ws, size_t ws_size,
                              hipStream_t stream) {
  rmt::run(d_in, d_out, d_ws, stream);
}
```

```cpp
#include <hip/hip_runtime.h>
#include <hip/hip_bf16.h>
#include <stdint.h>

namespace rmt {
typedef unsigned short u16;
typedef unsigned v4u __attribute__((ext_vector_type(4)));
typedef unsigned v2u __attribute__((ext_vector_type(2)));
typedef float f32x4 __attribute__((ext_vector_type(4)));
typedef short bf16x8 __attribute__((ext_vector_type(8)));
typedef __bf16 bfv2 __attribute__((ext_vector_type(2)));
#define SCHED_FENCE() __builtin_amdgcn_sched_barrier(0)
#define LAS __attribute__((address_space(3)))
#define GAS __attribute__((address_space(1)))

constexpr int S = 2048, DM = 640, H = 32, DH = 192, HD = H * DH, SEG = 32, NSEG = 64, DF = 1536;
constexpr int KS = DM / 32;
constexpr unsigned SENT = 0xFFFFFFFFu;
constexpr int AUX_SC1 = 16;

constexpr int XB_SLOTS = 64 * 65536;
constexpr int XB_CTL = XB_SLOTS;
constexpr int XB_POL = XB_CTL + 128, XB_PROG = XB_CTL + 256, XB_CNTA = XB_CTL + 384, XB_CNTB = XB_CTL + 512, XB_CNTALL = XB_CTL + 640,
              XB_CNTF = XB_CTL + 768;
constexpr int XB_XFLAG = XB_CTL + 1024;
constexpr int XB_KMFLAG = XB_XFLAG + 64 * 32 * 64;
constexpr int XB_VOCNT = XB_KMFLAG + 64 * 32 * 64;
constexpr int XB_PXCNT = XB_VOCNT + 64 * 128;
constexpr int XB_SIZE = XB_PXCNT + 64 * 128;
constexpr size_t OFF_XNF = (size_t)8 << 20;
constexpr size_t OFF_WF = OFF_XNF + (size_t)128 * 20 * 1024;
constexpr size_t OFF_WVF = OFF_WF + (size_t)32 * 24 * 20 * 1024;
constexpr size_t OFF_WGU = OFF_WVF + (size_t)80 * 16 * 20 * 1024;
constexpr size_t OFF_WD = OFF_WGU + (size_t)96 * 2 * 20 * 1024;
constexpr size_t OFF_QX = OFF_WD + (size_t)40 * 48 * 1024;
constexpr size_t OFF_KX = OFF_QX + (size_t)64 * 32 * 12 * 1024;
constexpr size_t OFF_KM = OFF_KX + (size_t)64 * 32 * 12 * 1024;
constexpr size_t OFF_PX = OFF_KM + (size_t)64 * 32 * 6 * 1024;
constexpr size_t OFF_VO = OFF_PX + (size_t)64 * 16 * 4 * 1536;
constexpr size_t OFF_H = OFF_VO + (size_t)64 * 80 * 16 * 1280;
constexpr size_t OFF_HNF = OFF_H + (size_t)S * DM * 4;
constexpr size_t OFF_AF = OFF_HNF + (size_t)128 * 20 * 1024;
constexpr size_t WS_END = OFF_AF + (size_t)128 * 48 * 1024;
static_assert((size_t)XB_SIZE + (2u << 20) <= OFF_XNF, "xb");
static_assert(WS_END <= (size_t)256 * 1024 * 1024, "workspace");

__device__ __forceinline__ float bflo(unsigned v) { return __uint_as_float(v << 16); }
__device__ __forceinline__ float bfhi(unsigned v) { return __uint_as_float(v & 0xFFFF0000u); }
__device__ __forceinline__ unsigned pack2(float a, float b) {
  bfv2 v = {(__bf16)a, (__bf16)b};
  return __builtin_bit_cast(unsigned, v);
}
__device__ __forceinline__ unsigned nosent(unsigned d) { return d == SENT ? 0x7FC07FC0u : d; }
__device__ __forceinline__ f32x4 mfma32(v4u a, v4u b, f32x4 c) {
  return __builtin_amdgcn_mfma_f32_16x16x32_bf16(__builtin_bit_cast(bf16x8, a), __builtin_bit_cast(bf16x8, b), c, 0, 0, 0);
}
__device__ __forceinline__ float xmax4(float x) {
  auto s = __builtin_amdgcn_permlane16_swap(__float_as_uint(x), __float_as_uint(x), false, false);
  x = fmaxf(__uint_as_float(s[0]), __uint_as_float(s[1]));
  auto t = __builtin_amdgcn_permlane32_swap(__float_as_uint(x), __float_as_uint(x), false, false);
  return fmaxf(__uint_as_float(t[0]), __uint_as_float(t[1]));
}
__device__ __forceinline__ float xsum4(float x) {
  auto s = __builtin_amdgcn_permlane16_swap(__float_as_uint(x), __float_as_uint(x), false, false);
  x = __uint_as_float(s[0]) + __uint_as_float(s[1]);
  auto t = __builtin_amdgcn_permlane32_swap(__float_as_uint(x), __float_as_uint(x), false, false);
  return __uint_as_float(t[0]) + __uint_as_float(t[1]);
}
__device__ __forceinline__ int xmap(int o) { return (o >> 13) * 16384 + (o & 8191); }
__device__ __forceinline__ bool valid4(v4u v) { return (v.x != SENT) & (v.y != SENT) & (v.z != SENT) & (v.w != SENT); }
__device__ __forceinline__ bool valid2(v2u v) { return (v.x != SENT) & (v.y != SENT); }

constexpr int NB_VO = 256, NB_WQ = 5 * 48, NB_WG = 5 * 12, NB_WD = 12 * 5;
constexpr int LN_BLOCKS = S / 16;
constexpr int NB_FILL = (XB_SIZE - 32768 + 65535) / 65536;
constexpr int PRE_GRID = NB_VO + 2 * NB_WQ + LN_BLOCKS + 1 + 2 * NB_WG + NB_WD + NB_FILL;

__device__ __forceinline__ void ln_rows_bf16(const u16* __restrict__ x, unsigned char* __restrict__ outF, int blk) {
  const int t = threadIdx.x, lane = t & 63, w = t >> 6;
  const int row = blk * 16 + w * 4 + (lane >> 4), q = lane & 15;
  float v[5][8];
  float s = 0.f;
#pragma unroll
  for (int i = 0; i < 5; ++i) {
    v4u d = *(const v4u*)(x + (size_t)row * DM + (q + 16 * i) * 8);
    v[i][0] = bflo(d.x); v[i][1] = bfhi(d.x); v[i][2] = bflo(d.y); v[i][3] = bfhi(d.y);
    v[i][4] = bflo(d.z); v[i][5] = bfhi(d.z); v[i][6] = bflo(d.w); v[i][7] = bfhi(d.w);
#pragma unroll
    for (int j = 0; j < 8; ++j) s += v[i][j];
  }
#pragma unroll
  for (int o = 1; o < 16; o <<= 1) s += __shfl_xor(s, o);
  const float mean = s * (1.f / DM);
  float qq = 0.f;
#pragma unroll
  for (int i = 0; i < 5; ++i)
#pragma unroll
    for (int j = 0; j < 8; ++j) { float d = v[i][j] - mean; qq += d * d; }
#pragma unroll
  for (int o = 1; o < 16; o <<= 1) qq += __shfl_xor(qq, o);
  const float rs = 1.f / sqrtf(qq * (1.f / DM) + 1e-5f);
#pragma unroll
  for (int i = 0; i < 5; ++i) {
    const int pc = q + 16 * i, kk = pc >> 2, g = pc & 3;
    v4u o;
    o.x = pack2((v[i][0] - mean) * rs, (v[i][1] - mean) * rs);
    o.y = pack2((v[i][2] - mean) * rs, (v[i][3] - mean) * rs);
    o.z = pack2((v[i][4] - mean) * rs, (v[i][5] - mean) * rs);
    o.w = pack2((v[i][6] - mean) * rs, (v[i][7] - mean) * rs);
    *(v4u*)(outF + ((size_t)(row >> 4) * 20 + kk) * 1024 + (g * 16 + (row & 15)) * 16) = o;
  }
}

__device__ __forceinline__ void pretile4(const u16* __restrict__ src, int ld, int kk0, int nb, int job, unsigned char* __restrict__ ws,
                                         LAS unsigned* tile) {
  const int t = threadIdx.x;
  {
    const int row = t >> 3, ch = t & 7;
    v4u a[4], c[4];
#pragma unroll
    for (int u = 0; u < 4; ++u) {
      const v4u* p = (const v4u*)(src + (size_t)((kk0 + u) * 32 + row) * ld + nb * 128 + ch * 16);
      a[u] = p[0]; c[u] = p[1];
    }
#pragma unroll
    for (int u = 0; u < 4; ++u) {
      LAS unsigned* d = tile + u * 2080 + row * 65 + ch * 8;
      d[0] = a[u].x; d[1] = a[u].y; d[2] = a[u].z; d[3] = a[u].w; d[4] = c[u].x; d[5] = c[u].y; d[6] = c[u].z; d[7] = c[u].w;
    }
  }
  __syncthreads();
#pragma unroll
  for (int u = 0; u < 4; ++u) {
    const LAS u16* tl = (const LAS u16*)(tile + u * 2080);
    const int kk = kk0 + u;
#pragma unroll
    for (int it = 0; it < 2; ++it) {
      const int pc = t + it * 256, i = pc >> 6, lane = pc & 63, g = lane >> 4, n = lane & 15;
      unsigned e[8];
#pragma unroll
      for (int j = 0; j < 8; ++j) {
        const int row = (job == 5) ? (16 * (j >> 2) + 4 * g + (j & 3)) : (8 * g + j);
        e[j] = tl[row * 130 + i * 16 + n];
      }
      v4u o = {e[0] | (e[1] << 16), e[2] | (e[3] << 16), e[4] | (e[5] << 16), e[6] | (e[7] << 16)};
      const int nt = nb * 8 + i;
      size_t dst;
      if (job <= 1) { const int h = nt / 12, et = nt % 12; dst = OFF_WF + ((size_t)(h * 24 + job * 12 + et) * 20 + kk) * 1024; }
      else if (job <= 4) dst = OFF_WGU + ((size_t)(nt * 2 + (job - 3)) * 20 + kk) * 1024;
      else dst = OFF_WD + ((size_t)nt * 48 + kk) * 1024;
      *(v4u*)(ws + dst + lane * 16) = o;
    }
  }
}

constexpr int PRE_LDS = 192 * 41 * 4 + 30 * 1024;
__global__ void __launch_bounds__(256) k_pre(const u16* __restrict__ x, const u16* __restrict__ mem0, const u16* __restrict__ Wq,
                                             const u16* __restrict__ Wk, const u16* __restrict__ Wv, const u16* __restrict__ Wo,
                                             const u16* __restrict__ wg, const u16* __restrict__ wu, const u16* __restrict__ wd,
                                             unsigned char* __restrict__ ws, unsigned char* __restrict__ xb) {
  __shared__ __attribute__((aligned(16))) unsigned char sm[PRE_LDS];
  LAS unsigned char* L = (LAS unsigned char*)sm;
  int b = blockIdx.x;
  const int t = threadIdx.x;
  if (b < NB_VO) {
    const int h = b >> 3, cq = b & 7;
    const int lane = t & 63, g = lane >> 4, n = lane & 15, w = __builtin_amdgcn_readfirstlane(t >> 6);
    LAS unsigned* raw = (LAS unsigned*)L;
    LAS unsigned char* bfr = L + 192 * 41 * 4;
    auto load_a = [&](int kk, v4u (&a)[12]) {
#pragma unroll
      for (int m = 0; m < 2; ++m)
#pragma unroll
        for (int ke = 0; ke < 6; ++ke)
          a[m * 6 + ke] = *(const v4u*)(Wv + (size_t)(32 * kk + 8 * (n >> 2) + 4 * m + (n & 3)) * HD + h * DH + ke * 32 + g * 8);
    };
    v4u a0[12], a1[12];
    load_a(w, a0);
    SCHED_FENCE();
    for (int pc = t; pc < 192 * 10; pc += 256) {
      const int e = pc / 10, ch = pc % 10;
      const v4u d = *(const v4u*)(Wo + (size_t)(h * DH + e) * DM + cq * 80 + ch * 8);
      LAS unsigned* dp = raw + e * 41 + ch * 4;
      dp[0] = d.x; dp[1] = d.y; dp[2] = d.z; dp[3] = d.w;
    }
    __syncthreads();
    {
      const LAS u16* tl = (const LAS u16*)raw;
      for (int pc = t; pc < 30 * 64; pc += 256) {
        const int f = pc >> 6, l2 = pc & 63, g2 = l2 >> 4, n2 = l2 & 15, ct = f / 6, ke = f % 6;
        unsigned e[8];
#pragma unroll
        for (int j = 0; j < 8; ++j) e[j] = tl[(ke * 32 + 8 * g2 + j) * 82 + ct * 16 + n2];
        *(LAS v4u*)(bfr + pc * 16) = v4u{e[0] | (e[1] << 16), e[2] | (e[3] << 16), e[4] | (e[5] << 16), e[6] | (e[7] << 16)};
      }
    }
    __syncthreads();
    const int hp = h >> 1, n2 = (h & 1) * 8 + (n & 7);
    auto body = [&](int kk, v4u (&a)[12]) {
#pragma unroll
      for (int ct = 0; ct < 5; ++ct) {
        f32x4 lo = {0.f, 0.f, 0.f, 0.f}, hi = {0.f, 0.f, 0.f, 0.f};
#pragma unroll
        for (int ke = 0; ke < 6; ++ke) {
          const v4u bf = *(LAS v4u*)(bfr + (ct * 6 + ke) * 1024 + lane * 16);
          lo = mfma32(a[ke], bf, lo); hi = mfma32(a[6 + ke], bf, hi);
        }
        const int c8 = 2 * (cq * 5 + ct) + (n >> 3);
        const v4u o = {pack2(lo[0], lo[1]), pack2(lo[2], lo[3]), pack2(hi[0], hi[1]), pack2(hi[2], hi[3])};
        *(v4u*)(ws + OFF_WVF + (((size_t)(c8 * 16 + hp) * 20 + kk) * 64 + g * 16 + n2) * 16) = o;
      }
    };
    load_a(w + 4, a1); SCHED_FENCE(); body(w, a0); SCHED_FENCE();
    load_a(w + 8, a0); SCHED_FENCE(); body(w + 4, a1); SCHED_FENCE();
    load_a(w + 12, a1); SCHED_FENCE(); body(w + 8, a0); SCHED_FENCE();
    load_a(w + 16, a0); SCHED_FENCE(); body(w + 12, a1); SCHED_FENCE();
    body(w + 16, a0);
    return;
  }
  b -= NB_VO;
  if (b < 2 * NB_WQ) {
    const int job = b >= NB_WQ; if (job) b -= NB_WQ;
    pretile4(job ? Wk : Wq, HD, (b / 48) * 4, b % 48, job, ws, (LAS unsigned*)L);
    return;
  }
  b -= 2 * NB_WQ;
  if (b < LN_BLOCKS) { ln_rows_bf16(x, ws + OFF_XNF, b); return; }
  b -= LN_BLOCKS;
  if (b < NB_WG) { pretile4(wg, DF, (b / 12) * 4, b % 12, 3, ws, (LAS unsigned*)L); return; }
  b -= NB_WG;
  if (b < NB_WG) { pretile4(wu, DF, (b / 12) * 4, b % 12, 4, ws, (LAS unsigned*)L); return; }
  b -= NB_WG;
  if (b < NB_WD) { pretile4(wd, DM, (b / 5) * 4, b % 5, 5, ws, (LAS unsigned*)L); return; }
  b -= NB_WD;
  const v4u ones = {SENT, SENT, SENT, SENT};
  if (b < NB_FILL) {
    const int base = 32768 + b * 65536;
#pragma unroll
    for (int i = 0; i < 16; ++i) { const int o = base + (i * 256 + t) * 16; if (o < XB_SIZE) *(v4u*)(xb + o) = ones; }
    return;
  }
  for (int pc = t; pc < 2048; pc += 256) {
    const int o = pc * 16;
    const int lo = (o < 8192) ? o : ((o >= 16384 && o < 24576) ? o - 8192 : -1);
    v4u d = ones;
    if (lo >= 0 && lo < 10240) {
      const int q = lo >> 4, c8 = q >> 3, i = q & 7;
      d = *(const v4u*)(mem0 + i * DM + c8 * 8);
      d.x = nosent(d.x); d.y = nosent(d.y); d.z = nosent(d.z); d.w = nosent(d.w);
    }
    *(v4u*)(xb + o) = d;
  }
}

constexpr int NSC = 32, NCOL = 80, NCH = NSC + NCOL, NXS = 64, NAO = 64, CH_T = 512, CH_GRID = 256;
constexpr unsigned SPIN_CAP = 1u << 21;
constexpr float QSCALE = 0.07216878364870322f * 1.4426950408889634f;
constexpr int SL_W = 0, SL_MEM = 112 * 1024, SL_QK = SL_MEM + 10240, SL_KX = SL_QK + 12 * 1024, SL_ROLE = SL_KX + 24 * 1024, SL_END = SL_ROLE + 64;
constexpr int CL_X = 0, CL_MEM = 80 * 1024, CL_PART = CL_MEM + 10240, CL_END = CL_PART + 4096;
constexpr int XL_X = 0, XL_MEM = 80 * 1024, XL_CNT = XL_MEM + 10240, XL_END = XL_CNT + 64;
constexpr int CH_LDS = SL_END;
static_assert(CH_LDS <= 160 * 1024 && CL_END <= SL_ROLE && XL_END <= SL_ROLE, "lds");

#define RAW_BARRIER() do { asm volatile("s_waitcnt lgkmcnt(0)" ::: "memory"); __builtin_amdgcn_s_barrier(); asm volatile("" ::: "memory"); } while (0)
__device__ __forceinline__ v4u ldsr4(LAS unsigned char* p) { return *(LAS v4u*)p; }
__device__ __forceinline__ float max3f(float a, float b, float c) { float r; asm("v_max3_f32 %0, %1, %2, %3" : "=v"(r) : "v"(a), "v"(b), "v"(c)); return r; }
__device__ __forceinline__ float max2f(float a, float b) { float r; asm("v_max_f32 %0, %1, %2" : "=v"(r) : "v"(a), "v"(b)); return r; }
__device__ __forceinline__ float ror8(float x) {
  return __builtin_bit_cast(float, __builtin_amdgcn_update_dpp(0, __builtin_bit_cast(int, x), 0x128, 0xf, 0xf, false));
}
__device__ __forceinline__ float xmax8(float x) {
  x = max2f(x, ror8(x));
  auto s = __builtin_amdgcn_permlane16_swap(__float_as_uint(x), __float_as_uint(x), false, false);
  x = max2f(__uint_as_float(s[0]), __uint_as_float(s[1]));
  auto t = __builtin_amdgcn_permlane32_swap(__float_as_uint(x), __float_as_uint(x), false, false);
  return max2f(__uint_as_float(t[0]), __uint_as_float(t[1]));
}
__device__ __forceinline__ float xsum8(float x) { x += ror8(x); return xsum4(x); }
__device__ __forceinline__ bool timed_out(__amdgpu_buffer_rsrc_t rs, unsigned spins) {
  if ((spins & 1023u) != 1023u) return false;
  const unsigned v = (unsigned)__builtin_amdgcn_readfirstlane((int)__builtin_amdgcn_raw_buffer_load_b32(rs, XB_CTL, 0, AUX_SC1));
  if (v == 1u) return true;
  if (spins < SPIN_CAP) return false;
  __builtin_amdgcn_raw_buffer_store_b32(1u, rs, XB_CTL, 0, AUX_SC1);
  return true;
}
constexpr int POL_FROM = 4;
__device__ __forceinline__ int slot_off(int s, unsigned pol) { return s * 65536 + (s >= POL_FROM ? (int)((pol >> (s >> 5)) & 1u) * 8192 : 0); }

__device__ __forceinline__ bool gather_mem(__amdgpu_buffer_rsrc_t rs, int soff, int w, int lane, LAS unsigned char* img) {
  const int p0 = 80 * w + lane, p1 = 80 * w + 64 + (lane & 15);
  const int o0 = xmap(p0 * 16), o1 = xmap(p1 * 16);
  v4u v0, v1;
  for (unsigned spins = 1023u;; spins += 1024u) {
    bool ok = false;
    for (int i = 0; i < 1024; ++i) {
      asm volatile("" ::: "memory");
      v0 = __builtin_amdgcn_raw_buffer_load_b128(rs, o0, soff, AUX_SC1);
      v1 = __builtin_amdgcn_raw_buffer_load_b128(rs, o1, soff, AUX_SC1);
      if (__all(valid4(v0) & valid4(v1))) { ok = true; break; }
    }
    if (ok) break;
    if (timed_out(rs, spins)) return false;
  }
  *(LAS v4u*)(img + p0 * 16) = v0;
  if (lane < 16) *(LAS v4u*)(img + p1 * 16) = v1;
  return true;
}
__device__ __forceinline__ bool wait_flags2(__amdgpu_buffer_rsrc_t rs, int off) {
  for (unsigned spins = 0;; ++spins) {
    asm volatile("" ::: "memory");
    v2u f = __builtin_amdgcn_raw_buffer_load_b64(rs, off, 0, AUX_SC1);
    if (__all(valid2(f))) break;
    if (timed_out(rs, spins)) return false;
    __builtin_amdgcn_s_sleep(8);
  }
  asm volatile("" ::: "memory");
  return true;
}
__device__ __forceinline__ unsigned wait_pol(__amdgpu_buffer_rsrc_t rs) {
  unsigned v;
  for (unsigned spins = 0;; ++spins) {
    asm volatile("" ::: "memory");
    v = __builtin_amdgcn_raw_buffer_load_b32(rs, XB_POL, 0, AUX_SC1);
    if (v != SENT) break;
    if (timed_out(rs, spins)) { v = 0; break; }
    __builtin_amdgcn_s_sleep(2);
  }
  return (unsigned)__builtin_amdgcn_readfirstlane((int)v);
}

__device__ __forceinline__ void score_role(unsigned char* __restrict__ ws, __amdgpu_buffer_rsrc_t rs, int h, LAS unsigned char* L) {
  const int tid = threadIdx.x, lane = tid & 63, g = lane >> 4, n = lane & 15;
  const int w = __builtin_amdgcn_readfirstlane(tid >> 6);
  const __amdgpu_buffer_rsrc_t rkx = __builtin_amdgcn_make_buffer_rsrc((void*)(ws + OFF_KX), 0, 64 * 32 * 12 * 1024, 0x00020000);
  v4u wr[46];
  {
    const unsigned char* src = ws + OFF_WF + ((size_t)(h * 24 + 3 * w) * 20) * 1024 + lane * 16;
#pragma unroll
    for (int i = 0; i < 46; ++i) wr[i] = *(const v4u*)(src + i * 1024);
#pragma unroll
    for (int i = 46; i < 60; ++i) *(LAS v4u*)(L + SL_W + (w * 14 + i - 46) * 1024 + lane * 16) = *(const v4u*)(src + i * 1024);
  }
  auto fetch_kx = [&](int seg) -> bool {
    if (!wait_flags2(rs, XB_XFLAG + (seg * 32 + h) * 64)) return false;
    const int src = ((seg * 32 + h) * 12) * 1024 + lane * 16;
    LAS unsigned char* dst = L + SL_KX + (seg & 1) * 12288 + lane * 16;
    const v4u a = __builtin_amdgcn_raw_buffer_load_b128(rkx, src + w * 1024, 0, AUX_SC1);
    const v4u b = __builtin_amdgcn_raw_buffer_load_b128(rkx, src + (8 + (w & 3)) * 1024, 0, AUX_SC1);
    *(LAS v4u*)(dst + w * 1024) = a;
    if (w < 4) *(LAS v4u*)(dst + (8 + w) * 1024) = b;
    return true;
  };
  unsigned pol = 0;
  if (!fetch_kx(0)) return;
  const float qs = w < 4 ? QSCALE : 1.f;
  LAS unsigned char* memrd = L + SL_MEM + (g * 8 + (n & 7)) * 16;
  LAS unsigned char* wl = L + SL_W + (w * 14) * 1024 + lane * 16;
  const int pox = xmap(10240 + h * 640 + (g * 8 + (n & 7)) * 16 + (n >> 3) * 8);
  const int pom = xmap(10240 + h * 640 + 512 + ((g & 1) * 8 + (n & 7)) * 8 + (n >> 3) * 4);
  for (int s = 0; s < NSEG - 1; ++s) {
    if (s == POL_FROM) pol = wait_pol(rs);
    const int soff = slot_off(s, pol);
    if (!gather_mem(rs, soff, w, lane, L + SL_MEM)) return;
    RAW_BARRIER();
    f32x4 acc[3] = {{0, 0, 0, 0}, {0, 0, 0, 0}, {0, 0, 0, 0}};
    {
      v4u bq[3], aq[2];
      bq[0] = ldsr4(memrd); bq[1] = ldsr4(memrd + 512); bq[2] = ldsr4(memrd + 1024);
      SCHED_FENCE();
#pragma unroll
      for (int kk = 0; kk < KS; ++kk) {
        const v4u b = bq[kk % 3];
        v4u a2;
        if (kk < 6) a2 = wr[40 + kk]; else a2 = aq[kk & 1];
        acc[0] = mfma32(wr[kk], b, acc[0]);
        acc[1] = mfma32(wr[20 + kk], b, acc[1]);
        acc[2] = mfma32(a2, b, acc[2]);
        if (kk + 3 < KS) bq[kk % 3] = ldsr4(memrd + (kk + 3) * 512);
        if (kk + 2 >= 6 && kk + 2 < KS) aq[kk & 1] = ldsr4(wl + (kk + 2 - 6) * 1024);
        SCHED_FENCE();
      }
    }
#pragma unroll
    for (int t = 0; t < 3; ++t) {
      const int tau = 3 * w + t;
      v2u o = {pack2(acc[t][0] * qs, acc[t][1] * qs), pack2(acc[t][2] * qs, acc[t][3] * qs)};
      *(LAS v2u*)(L + SL_QK + (tau >> 1) * 1024 + lane * 16 + (tau & 1) * 8) = o;
    }
    RAW_BARRIER();
    if (w == 0) {
      LAS unsigned char* kx = L + SL_KX + (s & 1) * 12288 + lane * 16;
      LAS unsigned char* qk = L + SL_QK + lane * 16;
      f32x4 sm = {0, 0, 0, 0}, s0 = {0, 0, 0, 0}, s1 = {0, 0, 0, 0};
      v4u fq[2], fm[2], f0[2], f1[2];
      fq[0] = ldsr4(qk); fm[0] = ldsr4(qk + 6 * 1024); f0[0] = ldsr4(kx); f1[0] = ldsr4(kx + 6 * 1024);
      fq[1] = ldsr4(qk + 1024); fm[1] = ldsr4(qk + 7 * 1024); f0[1] = ldsr4(kx + 1024); f1[1] = ldsr4(kx + 7 * 1024);
      SCHED_FENCE();
#pragma unroll
      for (int p = 0; p < 6; ++p) {
        const int b = p & 1;
        sm = mfma32(fm[b], fq[b], sm);
        s0 = mfma32(f0[b], fq[b], s0);
        s1 = mfma32(f1[b], fq[b], s1);
        if (p + 2 < 6) {
          fq[b] = ldsr4(qk + (p + 2) * 1024); fm[b] = ldsr4(qk + (8 + p) * 1024);
          f0[b] = ldsr4(kx + (p + 2) * 1024); f1[b] = ldsr4(kx + (8 + p) * 1024);
        }
        SCHED_FENCE();
      }
      const float NEG = -3.0e38f;
      const bool up = n >= 8;
      float v[6];
      v[0] = up ? s1[0] : s0[0]; v[1] = up ? s1[1] : s0[1]; v[2] = up ? s1[2] : s0[2]; v[3] = up ? s1[3] : s0[3];
      v[4] = up ? sm[2] : sm[0]; v[5] = up ? sm[3] : sm[1];
      if (g >= 2) { v[4] = NEG; v[5] = NEG; }
      float mx = xmax8(max2f(max3f(v[0], v[1], v[2]), max3f(v[3], v[4], v[5])));
      float sum = 0.f;
#pragma unroll
      for (int r = 0; r < 6; ++r) { v[r] = __builtin_amdgcn_exp2f(v[r] - mx); sum += v[r]; }
      const float inv = __builtin_amdgcn_rcpf(xsum8(sum));
      v2u px = {nosent(pack2(v[0] * inv, v[1] * inv)), nosent(pack2(v[2] * inv, v[3] * inv))};
      const unsigned pm = nosent(pack2(v[4] * inv, v[5] * inv));
      __builtin_amdgcn_raw_buffer_store_b64(px, rs, pox, soff, AUX_SC1);
      if (g < 2) __builtin_amdgcn_raw_buffer_store_b32(pm, rs, pom, soff, AUX_SC1);
    }
    if (s + 1 < NSEG - 1) { if (!fetch_kx(s + 1)) return; }
  }
}

__device__ __forceinline__ void col_role(unsigned char* __restrict__ ws, unsigned char* __restrict__ xb, __amdgpu_buffer_rsrc_t rs, int c8,
                                         LAS unsigned char* L) {
  const int tid = threadIdx.x, lane = tid & 63, g = lane >> 4, n = lane & 15;
  const int w = __builtin_amdgcn_readfirstlane(tid >> 6);
  const __amdgpu_buffer_rsrc_t rvo = __builtin_amdgcn_make_buffer_rsrc((void*)(ws + OFF_VO), 0, 64 * 80 * 16 * 1280, 0x00020000);
  v4u wr[40];
  {
    const unsigned char* src = ws + OFF_WVF + ((size_t)(c8 * 16 + 2 * w) * 20) * 1024 + lane * 16;
#pragma unroll
    for (int i = 0; i < 40; ++i) wr[i] = *(const v4u*)(src + i * 1024);
  }
  auto stage_x = [&](int seg) {
    const unsigned char* src = ws + OFF_XNF + (size_t)seg * 40960 + w * 5120 + lane * 16;
    LAS unsigned char* dst = L + CL_X + (seg & 1) * 40960 + w * 5120;
#pragma unroll
    for (int i = 0; i < 5; ++i)
      __builtin_amdgcn_global_load_lds((const GAS void*)(src + i * 1024), (LAS void*)(dst + i * 1024), 16, 0, 0);
  };
  stage_x(0);
  stage_x(1);
  __syncthreads();
  v4u vox[2]; v2u vom[2];
  auto compute_vox = [&](int seg) {
    LAS unsigned char* xr = L + CL_X + (seg & 1) * 40960 + lane * 16;
    f32x4 a00 = {0, 0, 0, 0}, a01 = {0, 0, 0, 0}, a10 = {0, 0, 0, 0}, a11 = {0, 0, 0, 0};
    v4u x0[2], x1[2];
    x0[0] = ldsr4(xr); x1[0] = ldsr4(xr + 20 * 1024); x0[1] = ldsr4(xr + 1024); x1[1] = ldsr4(xr + 21 * 1024);
    SCHED_FENCE();
#pragma unroll
    for (int kk = 0; kk < KS; ++kk) {
      const int b = kk & 1;
      a00 = mfma32(x0[b], wr[kk], a00); a01 = mfma32(x1[b], wr[kk], a01);
      a10 = mfma32(x0[b], wr[20 + kk], a10); a11 = mfma32(x1[b], wr[20 + kk], a11);
      if (kk + 2 < KS) { x0[b] = ldsr4(xr + (kk + 2) * 1024); x1[b] = ldsr4(xr + (22 + kk) * 1024); }
      SCHED_FENCE();
    }
    vox[0] = v4u{pack2(a00[0], a00[1]), pack2(a00[2], a00[3]), pack2(a01[0], a01[1]), pack2(a01[2], a01[3])};
    vox[1] = v4u{pack2(a10[0], a10[1]), pack2(a10[2], a10[3]), pack2(a11[0], a11[1]), pack2(a11[2], a11[3])};
  };
  auto store_vo = [&](int seg, const v4u (&vx)[2]) {
    const int dst = ((seg * 80 + c8) * 16 + 2 * w) * 1280;
#pragma unroll
    for (int tt = 0; tt < 2; ++tt) {
      __builtin_amdgcn_raw_buffer_store_b128(vx[tt], rvo, dst + tt * 1280 + lane * 16, 0, AUX_SC1);
      if (g < 2) __builtin_amdgcn_raw_buffer_store_b64(vom[tt], rvo, dst + tt * 1280 + 1024 + lane * 8, 0, AUX_SC1);
    }
  };
  compute_vox(0);
  unsigned pol = 0;
  LAS unsigned char* memrd = L + CL_MEM + (g * 8 + (n & 7)) * 16;
  const int oa = xmap(10240 + (4 * w + (n >> 3)) * 640 + (g * 8 + (n & 7)) * 16);
  const int ob = xmap(10240 + (4 * w + 2 + (n >> 3)) * 640 + (g * 8 + (n & 7)) * 16);
  const int om = xmap(10240 + (4 * w + 2 * (g >> 1) + (n >> 3)) * 640 + 512 + ((g & 1) * 8 + (n & 7)) * 8);
  const int opub = xmap(c8 * 128 + n * 8);
  for (int s = 0; s < NSEG; ++s) {
    if (s == POL_FROM - 1) pol = wait_pol(rs);
    const int soff = slot_off(s, pol);
    if (!gather_mem(rs, soff, w, lane, L + CL_MEM)) return;
    __syncthreads();
    if (s >= 1 && tid == 128) __hip_atomic_fetch_add((GAS unsigned*)(xb + XB_VOCNT + (s - 1) * 128), 1u, __ATOMIC_RELAXED, __HIP_MEMORY_SCOPE_AGENT);
    if (s >= 1 && s + 1 < NSEG) stage_x(s + 1);
    if (c8 == 0 && tid == 64) __builtin_amdgcn_raw_buffer_store_b32((unsigned)(s + 1), rs, XB_PROG, 0, AUX_SC1);
    {
      f32x4 m0 = {0, 0, 0, 0}, m1 = {0, 0, 0, 0};
      v4u aq[3];
      aq[0] = ldsr4(memrd); aq[1] = ldsr4(memrd + 512); aq[2] = ldsr4(memrd + 1024);
      SCHED_FENCE();
#pragma unroll
      for (int kk = 0; kk < KS; ++kk) {
        const v4u a = aq[kk % 3];
        m0 = mfma32(a, wr[kk], m0); m1 = mfma32(a, wr[20 + kk], m1);
        if (kk + 3 < KS) aq[kk % 3] = ldsr4(memrd + (kk + 3) * 512);
        SCHED_FENCE();
      }
      vom[0] = v2u{pack2(m0[0], m0[1]), pack2(m0[2], m0[3])};
      vom[1] = v2u{pack2(m1[0], m1[1]), pack2(m1[2], m1[3])};
    }
    if (s == NSEG - 1) {
      store_vo(s, vox);
      asm volatile("s_waitcnt vmcnt(0)" ::: "memory");
      RAW_BARRIER();
      if (tid == 64) __hip_atomic_fetch_add((GAS unsigned*)(xb + XB_VOCNT + s * 128), 1u, __ATOMIC_RELAXED, __HIP_MEMORY_SCOPE_AGENT);
      break;
    }
    v4u pa, pb; v2u pmv;
    for (unsigned spins = 1023u;; spins += 1024u) {
      bool ok = false;
      for (int i = 0; i < 1024; ++i) {
        asm volatile("" ::: "memory");
        pa = __builtin_amdgcn_raw_buffer_load_b128(rs, oa, soff, AUX_SC1);
        pb = __builtin_amdgcn_raw_buffer_load_b128(rs, ob, soff, AUX_SC1);
        pmv = __builtin_amdgcn_raw_buffer_load_b64(rs, om, soff, AUX_SC1);
        if (__all(valid4(pa) & valid4(pb) & valid2(pmv))) { ok = true; break; }
      }
      if (ok) break;
      if (timed_out(rs, spins)) return;
    }
    {
      const v2u z = {0u, 0u};
      const v2u pm0 = g < 2 ? pmv : z, pm1 = g < 2 ? z : pmv;
      f32x4 d0 = {0, 0, 0, 0}, d1 = {0, 0, 0, 0}, d2 = {0, 0, 0, 0};
      d0 = mfma32(vox[0], pa, d0); d1 = mfma32(vox[1], pb, d1);
      d2 = mfma32(v4u{vom[0].x, vom[0].y, vom[1].x, vom[1].y}, v4u{pm0.x, pm0.y, pm1.x, pm1.y}, d2);
      d0 += d1 + d2;
      if ((g >> 1) == (n >> 3)) *(LAS f32x4*)(L + CL_PART + w * 512 + (n >> 3) * 256 + (n & 7) * 32 + (g & 1) * 16) = d0;
    }
    RAW_BARRIER();
    if (w == 0) {
      LAS unsigned char* pr = L + CL_PART + g * 1024 + n * 16;
      f32x4 t = (*(LAS f32x4*)(pr) + *(LAS f32x4*)(pr + 256)) + (*(LAS f32x4*)(pr + 512) + *(LAS f32x4*)(pr + 768));
      t[0] = xsum4(t[0]); t[1] = xsum4(t[1]); t[2] = xsum4(t[2]); t[3] = xsum4(t[3]);
      v2u o = {nosent(pack2(t[0], t[1])), nosent(pack2(t[2], t[3]))};
      if (g == 0) __builtin_amdgcn_raw_buffer_store_b64(o, rs, opub, slot_off(s + 1, pol), AUX_SC1);
    }
    SCHED_FENCE();
    RAW_BARRIER();
    const v4u vold[2] = {vox[0], vox[1]};
    compute_vox(s + 1);
    SCHED_FENCE();
    store_vo(s, vold);
  }
}

__device__ __forceinline__ void lds_sync6(volatile LAS unsigned* cnt, unsigned target, int lane) {
  asm volatile("s_waitcnt lgkmcnt(0)" ::: "memory");
  if (lane == 0) __hip_atomic_fetch_add((LAS unsigned*)cnt, 1u, __ATOMIC_RELAXED, __HIP_MEMORY_SCOPE_WORKGROUP);
  while (*cnt < target) __builtin_amdgcn_s_sleep(1);
  asm volatile("" ::: "memory");
}

__device__ __forceinline__ void xs_role(unsigned char* __restrict__ ws, unsigned char* __restrict__ xb, __amdgpu_buffer_rsrc_t rs, int h, int sp,
                                        LAS unsigned char* L) {
  const int tid = threadIdx.x, lane = tid & 63, g = lane >> 4, n = lane & 15;
  const int w = __builtin_amdgcn_readfirstlane(tid >> 6);
  const __amdgpu_buffer_rsrc_t rq = __builtin_amdgcn_make_buffer_rsrc((void*)(ws + OFF_QX), 0, (int)(OFF_VO - OFF_QX), 0x00020000);
  constexpr int RO_KX = (int)(OFF_KX - OFF_QX), RO_KM = (int)(OFF_KM - OFF_QX), RO_PX = (int)(OFF_PX - OFF_QX);
  volatile LAS unsigned* cnt = (volatile LAS unsigned*)(L + XL_CNT);
  const bool isk = w < 3;
  const int pl = isk ? w : (w == 3 ? 2 : (w - 4) & 1);
  v4u wr[40];
  if (w < 6) {
    const unsigned char* src = ws + OFF_WF + ((size_t)(h * 24 + (isk ? 12 : 0) + 6 * sp + 2 * pl) * 20) * 1024 + lane * 16;
#pragma unroll
    for (int i = 0; i < 40; ++i) wr[i] = *(const v4u*)(src + i * 1024);
  } else {
#pragma unroll
    for (int i = 0; i < 40; ++i) wr[i] = v4u{0u, 0u, 0u, 0u};
  }
  if (tid == 0) cnt[0] = 0u;
  auto stage_x = [&](int seg) {
    const unsigned char* src = ws + OFF_XNF + (size_t)seg * 40960 + (w - 6) * 20480 + lane * 16;
    LAS unsigned char* dst = L + XL_X + (seg & 1) * 40960 + (w - 6) * 20480;
#pragma unroll
    for (int i = 0; i < 20; ++i)
      __builtin_amdgcn_global_load_lds((const GAS void*)(src + i * 1024), (LAS void*)(dst + i * 1024), 16, 0, 0);
  };
  if (w >= 6) stage_x(0);
  asm volatile("s_waitcnt vmcnt(0)" ::: "memory");
  const float sc = isk ? 1.f : QSCALE;
  for (int s = 0; s < NSEG + 2; ++s) {
    RAW_BARRIER();
    if (s >= 2 && tid == 448) __builtin_amdgcn_raw_buffer_store_b32(1u, rs, XB_XFLAG + ((s - 2) * 32 + h) * 64 + sp * 4, 0, AUX_SC1);
    if (w >= 6) {
      if (s + 1 < NSEG) stage_x(s + 1);
      asm volatile("s_waitcnt vmcnt(0)" ::: "memory");
    } else if (s < NSEG) {
      LAS unsigned char* xr = L + XL_X + (s & 1) * 40960 + lane * 16;
      f32x4 a00 = {0, 0, 0, 0}, a01 = {0, 0, 0, 0}, a10 = {0, 0, 0, 0}, a11 = {0, 0, 0, 0};
      v4u x0[2], x1[2];
      x0[0] = ldsr4(xr); x1[0] = ldsr4(xr + 20 * 1024); x0[1] = ldsr4(xr + 1024); x1[1] = ldsr4(xr + 21 * 1024);
      SCHED_FENCE();
#pragma unroll
      for (int kk = 0; kk < KS; ++kk) {
        const int b = kk & 1;
        a00 = mfma32(wr[kk], x0[b], a00); a01 = mfma32(wr[kk], x1[b], a01);
        a10 = mfma32(wr[20 + kk], x0[b], a10); a11 = mfma32(wr[20 + kk], x1[b], a11);
        if (kk + 2 < KS) { x0[b] = ldsr4(xr + (kk + 2) * 1024); x1[b] = ldsr4(xr + (22 + kk) * 1024); }
        SCHED_FENCE();
      }
      const v4u o0 = {pack2(a00[0] * sc, a00[1] * sc), pack2(a00[2] * sc, a00[3] * sc), pack2(a10[0] * sc, a10[1] * sc), pack2(a10[2] * sc, a10[3] * sc)};
      const v4u o1 = {pack2(a01[0] * sc, a01[1] * sc), pack2(a01[2] * sc, a01[3] * sc), pack2(a11[0] * sc, a11[1] * sc), pack2(a11[2] * sc, a11[3] * sc)};
      const int dst = (isk ? RO_KX : 0) + ((s * 32 + h) * 12 + 3 * sp + pl) * 1024 + lane * 16;
      __builtin_amdgcn_raw_buffer_store_b128(o0, rq, dst, 0, AUX_SC1);
      __builtin_amdgcn_raw_buffer_store_b128(o1, rq, dst + 6 * 1024, 0, AUX_SC1);
      asm volatile("s_waitcnt vmcnt(2)" ::: "memory");
    } else {
      asm volatile("s_waitcnt vmcnt(0)" ::: "memory");
    }
  }
  const unsigned pol = wait_pol(rs);
  if (w < 6) {
    LAS unsigned char* memrd = L + XL_MEM + (g * 8 + (n & 7)) * 16;
    const int p0 = 128 * w + lane, o0 = xmap(p0 * 16), o1 = xmap(p0 * 16 + 1024);
    for (int s = 0; s < NSEG; ++s) {
      if (w < 5) {
        for (unsigned spins = 0;; ++spins) {
          asm volatile("" ::: "memory");
          const unsigned pr = __builtin_amdgcn_raw_buffer_load_b32(rs, XB_PROG, 0, AUX_SC1);
          if (pr != SENT && pr > (unsigned)s) break;
          if (timed_out(rs, spins)) break;
          __builtin_amdgcn_s_sleep(16);
        }
        const int soff = slot_off(s, pol);
        v4u v0, v1;
        for (unsigned spins = 0;; ++spins) {
          asm volatile("" ::: "memory");
          v0 = __builtin_amdgcn_raw_buffer_load_b128(rs, o0, soff, AUX_SC1);
          v1 = __builtin_amdgcn_raw_buffer_load_b128(rs, o1, soff, AUX_SC1);
          if (__all(valid4(v0) & valid4(v1))) break;
          if (timed_out(rs, spins)) break;
          __builtin_amdgcn_s_sleep(8);
        }
        *(LAS v4u*)(L + XL_MEM + p0 * 16) = v0;
        *(LAS v4u*)(L + XL_MEM + p0 * 16 + 1024) = v1;
      }
      lds_sync6(cnt, (unsigned)(12 * s + 6), lane);
      if (isk) {
        f32x4 m0 = {0, 0, 0, 0}, m1 = {0, 0, 0, 0};
        v4u aq[3];
        aq[0] = ldsr4(memrd); aq[1] = ldsr4(memrd + 512); aq[2] = ldsr4(memrd + 1024);
        SCHED_FENCE();
#pragma unroll
        for (int kk = 0; kk < KS; ++kk) {
          const v4u b = aq[kk % 3];
          m0 = mfma32(wr[kk], b, m0); m1 = mfma32(wr[20 + kk], b, m1);
          if (kk + 3 < KS) aq[kk % 3] = ldsr4(memrd + (kk + 3) * 512);
          SCHED_FENCE();
        }
        const v4u o = {pack2(m0[0], m0[1]), pack2(m0[2], m0[3]), pack2(m1[0], m1[1]), pack2(m1[2], m1[3])};
        __builtin_amdgcn_raw_buffer_store_b128(o, rq, RO_KM + ((s * 32 + h) * 6 + 3 * sp + pl) * 1024 + lane * 16, 0, AUX_SC1);
        asm volatile("s_waitcnt vmcnt(0)" ::: "memory");
      }
      lds_sync6(cnt, (unsigned)(12 * s + 12), lane);
      if (tid == 0) __builtin_amdgcn_raw_buffer_store_b32(1u, rs, XB_KMFLAG + (s * 32 + h) * 64 + sp * 4, 0, AUX_SC1);
    }
  } else {
    const int rt = sp;
    for (int s = (w & 1); s < NSEG; s += 2) {
      if (!wait_flags2(rs, XB_XFLAG + (s * 32 + h) * 64)) return;
      const int qo = ((s * 32 + h) * 12 + rt * 6) * 1024 + lane * 16;
      const int ko = RO_KX + ((s * 32 + h) * 12) * 1024 + lane * 16;
      v4u fq[6], f0[6], f1[6], fm[6];
#pragma unroll
      for (int p = 0; p < 6; ++p) {
        fq[p] = __builtin_amdgcn_raw_buffer_load_b128(rq, qo + p * 1024, 0, AUX_SC1);
        f0[p] = __builtin_amdgcn_raw_buffer_load_b128(rq, ko + p * 1024, 0, AUX_SC1);
        f1[p] = __builtin_amdgcn_raw_buffer_load_b128(rq, ko + (6 + p) * 1024, 0, AUX_SC1);
      }
      f32x4 sm = {0, 0, 0, 0}, s0 = {0, 0, 0, 0}, s1 = {0, 0, 0, 0};
#pragma unroll
      for (int p = 0; p < 6; ++p) { s0 = mfma32(f0[p], fq[p], s0); s1 = mfma32(f1[p], fq[p], s1); }
      {
        const unsigned need = (unsigned)(12 * (s + 1 < NSEG ? s + 1 : s) + 12);
        unsigned spins = 0;
        while (*cnt < need) { __builtin_amdgcn_s_sleep(8); if (timed_out(rs, ++spins)) break; }
      }
      if (!wait_flags2(rs, XB_KMFLAG + (s * 32 + h) * 64)) return;
      const int mo = RO_KM + ((s * 32 + h) * 6) * 1024 + lane * 16;
#pragma unroll
      for (int p = 0; p < 6; ++p) fm[p] = __builtin_amdgcn_raw_buffer_load_b128(rq, mo + p * 1024, 0, AUX_SC1);
#pragma unroll
      for (int p = 0; p < 6; ++p) sm = mfma32(fm[p], fq[p], sm);
      const float NEG = -3.0e38f;
      const int t = rt * 16 + n;
      if (g >= 2) sm = f32x4{NEG, NEG, NEG, NEG};
#pragma unroll
      for (int r = 0; r < 4; ++r) {
        if (4 * g + r > t) s0[r] = NEG;
        if (16 + 4 * g + r > t) s1[r] = NEG;
      }
      float mx = max3f(max3f(sm[0], sm[1], sm[2]), max3f(sm[3], s0[0], s0[1]), max3f(max3f(s0[2], s0[3], s1[0]), s1[1], max2f(s1[2], s1[3])));
      mx = xmax4(mx);
      float em[4], e0[4], e1[4], sum = 0.f;
#pragma unroll
      for (int r = 0; r < 4; ++r) {
        em[r] = __builtin_amdgcn_exp2f(sm[r] - mx); e0[r] = __builtin_amdgcn_exp2f(s0[r] - mx); e1[r] = __builtin_amdgcn_exp2f(s1[r] - mx);
        sum += em[r] + e0[r] + e1[r];
      }
      const float inv = 1.f / xsum4(sum);
      const v4u px = {pack2(e0[0] * inv, e0[1] * inv), pack2(e0[2] * inv, e0[3] * inv), pack2(e1[0] * inv, e1[1] * inv), pack2(e1[2] * inv, e1[3] * inv)};
      const v2u pm = {pack2(em[0] * inv, em[1] * inv), pack2(em[2] * inv, em[3] * inv)};
      const int rg = 2 * rt + (n >> 3), l2 = g * 16 + (h & 1) * 8 + (n & 7);
      const int dst = RO_PX + ((s * 16 + (h >> 1)) * 4 + rg) * 1536;
      __builtin_amdgcn_raw_buffer_store_b128(px, rq, dst + l2 * 16, 0, AUX_SC1);
      __builtin_amdgcn_raw_buffer_store_b64(pm, rq, dst + 1024 + l2 * 8, 0, AUX_SC1);
      asm volatile("s_waitcnt vmcnt(0)" ::: "memory");
      if (lane == 0) __hip_atomic_fetch_add((GAS unsigned*)(xb + XB_PXCNT + s * 128), 1u, __ATOMIC_RELAXED, __HIP_MEMORY_SCOPE_AGENT);
    }
  }
}

__device__ __forceinline__ void ao_role(unsigned char* __restrict__ ws, __amdgpu_buffer_rsrc_t rs, int j, LAS unsigned char* L) {
  const int tid = threadIdx.x, lane = tid & 63, g = lane >> 4, n = lane & 15;
  const int w = __builtin_amdgcn_readfirstlane(tid >> 6);
  float* hout = (float*)(ws + OFF_H);
  for (int u = j; u < NSEG * 10; u += NAO) {
    const int s = u / 10, f = u % 10;
    if (w == 0) {
      for (unsigned spins = 0;; ++spins) {
        asm volatile("" ::: "memory");
        const unsigned a = __builtin_amdgcn_raw_buffer_load_b32(rs, XB_VOCNT + s * 128, 0, AUX_SC1);
        const unsigned b = __builtin_amdgcn_raw_buffer_load_b32(rs, XB_PXCNT + s * 128, 0, AUX_SC1);
        if (a == (unsigned)(NCOL - 1) && b == 63u) break;
        if (timed_out(rs, spins)) break;
        __builtin_amdgcn_s_sleep(16);
      }
      __builtin_amdgcn_fence(__ATOMIC_ACQUIRE, "agent");
      asm volatile("s_waitcnt vmcnt(0)" ::: "memory");
    }
    __syncthreads();
    {
      const unsigned char* src = ws + OFF_PX + (size_t)s * 98304 + w * 12288 + lane * 16;
#pragma unroll
      for (int i = 0; i < 12; ++i)
        __builtin_amdgcn_global_load_lds((const GAS void*)(src + i * 1024), (LAS void*)(L + w * 12288 + i * 1024), 16, 0, 0);
    }
    const int c0 = f * 8 + w;
    const unsigned char* vsrc = ws + OFF_VO + ((size_t)(s * 80 + c0) * 16) * 1280;
    v4u vx[16]; v2u vm[16];
#pragma unroll
    for (int k = 0; k < 16; ++k) {
      vx[k] = *(const v4u*)(vsrc + k * 1280 + lane * 16);
      vm[k] = *(const v2u*)(vsrc + k * 1280 + 1024 + (lane & 31) * 8);
    }
    SCHED_FENCE();
    __syncthreads();
    f32x4 acc[4] = {{0, 0, 0, 0}, {0, 0, 0, 0}, {0, 0, 0, 0}, {0, 0, 0, 0}};
#pragma unroll
    for (int hq = 0; hq < 8; ++hq) {
      const v4u vmm = {vm[2 * hq].x, vm[2 * hq].y, vm[2 * hq + 1].x, vm[2 * hq + 1].y};
#pragma unroll
      for (int rg = 0; rg < 4; ++rg) {
        const v4u px0 = *(LAS v4u*)(L + (hq * 8 + rg) * 1536 + lane * 16);
        const v4u px1 = *(LAS v4u*)(L + (hq * 8 + 4 + rg) * 1536 + lane * 16);
        const v2u pm0 = *(LAS v2u*)(L + (hq * 8 + rg) * 1536 + 1024 + lane * 8);
        const v2u pm1 = *(LAS v2u*)(L + (hq * 8 + 4 + rg) * 1536 + 1024 + lane * 8);
        acc[rg] = mfma32(vx[2 * hq], px0, acc[rg]);
        acc[rg] = mfma32(vx[2 * hq + 1], px1, acc[rg]);
        acc[rg] = mfma32(vmm, v4u{pm0.x, pm0.y, pm1.x, pm1.y}, acc[rg]);
      }
    }
#pragma unroll
    for (int rg = 0; rg < 4; ++rg) {
      f32x4 o;
#pragma unroll
      for (int r = 0; r < 4; ++r) o[r] = acc[rg][r] + __shfl(acc[rg][r], (lane + 40) & 63);
      if (g < 2 && n < 8) *(f32x4*)(hout + (size_t)(s * 32 + rg * 8 + n) * DM + c0 * 8 + g * 4) = o;
    }
  }
}

__global__ void __launch_bounds__(CH_T, 2) k_chain(unsigned char* __restrict__ ws, unsigned char* __restrict__ xb) {
  extern __shared__ __attribute__((aligned(16))) unsigned char smem[];
  LAS unsigned char* L = (LAS unsigned char*)smem;
  const __amdgpu_buffer_rsrc_t rs = __builtin_amdgcn_make_buffer_rsrc((void*)xb, 0, XB_SIZE, 0x00020000);
  if (threadIdx.x == 0) {
    GAS unsigned* cA = (GAS unsigned*)(xb + XB_CNTA); GAS unsigned* cB = (GAS unsigned*)(xb + XB_CNTB);
    GAS unsigned* cAll = (GAS unsigned*)(xb + XB_CNTALL); GAS unsigned* cF = (GAS unsigned*)(xb + XB_CNTF);
    unsigned xcc; asm volatile("s_getreg_b32 %0, hwreg(HW_REG_XCC_ID)" : "=s"(xcc));
    int role = -1;
    if ((xcc & 0xf) < 4) {
      const unsigned t = __hip_atomic_fetch_add(cA, 1u, __ATOMIC_RELAXED, __HIP_MEMORY_SCOPE_AGENT) + 1u;
      if (t <= (unsigned)NCH) role = (int)t;
    }
    if (role < 0) {
      const unsigned t = __hip_atomic_fetch_add(cB, 1u, __ATOMIC_RELAXED, __HIP_MEMORY_SCOPE_AGENT) + 1u;
      if (t < (unsigned)(NXS + NAO)) role = NCH + 1 + (int)t;
    }
    asm volatile("s_waitcnt vmcnt(0)" ::: "memory");
    __hip_atomic_fetch_add(cAll, 1u, __ATOMIC_RELAXED, __HIP_MEMORY_SCOPE_AGENT);
    if (role < 0) {
      for (unsigned spins = 0; spins < SPIN_CAP; ++spins) {
        const unsigned na = __hip_atomic_load(cA, __ATOMIC_RELAXED, __HIP_MEMORY_SCOPE_AGENT) + 1u;
        if (na > (unsigned)NCH) break;
        const unsigned nall = __hip_atomic_load(cAll, __ATOMIC_RELAXED, __HIP_MEMORY_SCOPE_AGENT) + 1u;
        if (nall >= (unsigned)CH_GRID) {
          const unsigned na2 = __hip_atomic_load(cA, __ATOMIC_RELAXED, __HIP_MEMORY_SCOPE_AGENT) + 1u;
          if (na2 <= (unsigned)NCH) {
            const unsigned t = __hip_atomic_fetch_add(cF, 1u, __ATOMIC_RELAXED, __HIP_MEMORY_SCOPE_AGENT) + 1u;
            if (na2 + t <= (unsigned)NCH) role = (int)(na2 + t);
          }
          break;
        }
        __builtin_amdgcn_s_sleep(8);
      }
    }
    *(LAS int*)(L + SL_ROLE) = role;
  }
  __syncthreads();
  const int role = __builtin_amdgcn_readfirstlane(*(LAS int*)(L + SL_ROLE));
  if (role < 0) return;
  if (role < NCH) {
    const int grp = role / 7, r = role % 7;
    if (r < 2) score_role(ws, rs, grp * 2 + r, L);
    else col_role(ws, xb, rs, grp * 5 + r - 2, L);
  } else if (role == NCH) {
    if (threadIdx.x < 64) {
      int best[4] = {0x7fffffff, 0x7fffffff, 0x7fffffff, 0x7fffffff};
      for (int it = 0; it < 7; ++it) {
#pragma unroll
        for (int c = 0; c < 4; ++c) {
          const int off = (c >> 1) * 32 * 65536 + xmap(31 * 1024) + (c & 1) * 8192;
          const long long t0 = __builtin_amdgcn_s_memtime();
          __builtin_amdgcn_raw_buffer_store_b32((unsigned)it, rs, off, 0, AUX_SC1);
          asm volatile("s_waitcnt vmcnt(0)" ::: "memory");
          unsigned v = __builtin_amdgcn_raw_buffer_load_b32(rs, off, 0, AUX_SC1);
          asm volatile("s_waitcnt vmcnt(0)" : "+v"(v) :: "memory");
          const int dt = (int)(__builtin_amdgcn_s_memtime() - t0);
          best[c] = dt < best[c] ? dt : best[c];
        }
      }
      const unsigned pv = (best[1] < best[0] ? 1u : 0u) | (best[3] < best[2] ? 2u : 0u);
      if (threadIdx.x == 0) __builtin_amdgcn_raw_buffer_store_b32(pv, rs, XB_POL, 0, AUX_SC1);
    }
  } else {
    if (role < NCH + 1 + NXS) xs_role(ws, xb, rs, (role - NCH - 1) >> 1, (role - NCH - 1) & 1, L);
    else ao_role(ws, rs, role - (NCH + 1 + NXS), L);
  }
}

__device__ __forceinline__ float gelu_tanh(float x) {
  const float u = 0.7978845608028654f * (x + 0.044715f * x * x * x);
  const float e = __expf(2.f * u);
  const float th = 1.f - 2.f / (e + 1.f);
  return 0.5f * x * (1.f + th);
}
__global__ void __launch_bounds__(512) k_ffn1(const u16* __restrict__ x, unsigned char* __restrict__ ws) {
  extern __shared__ __attribute__((aligned(16))) unsigned char smem[];
  LAS unsigned char* L = (LAS unsigned char*)smem;
  const int tid = threadIdx.x, lane = tid & 63, w = __builtin_amdgcn_readfirstlane(tid >> 6);
  const int rb = blockIdx.x & 31, nbk = blockIdx.x >> 5;
  const int fp = nbk * 8 + w;
  const unsigned char* wsrc = ws + OFF_WGU + ((size_t)(fp * 2) * 2 * 20) * 1024 + lane * 16;
  v4u wq[3][4];
#pragma unroll
  for (int k = 0; k < 3; ++k)
#pragma unroll
    for (int i = 0; i < 4; ++i) wq[k][i] = *(const v4u*)(wsrc + (i * 20 + k) * 1024);
  SCHED_FENCE();
#pragma unroll
  for (int ps = 0; ps < 2; ++ps) {
    const int rl = w * 8 + ps * 4 + (lane >> 4), q = lane & 15;
    const float* hsrc = (const float*)(ws + OFF_H) + (size_t)(rb * 64 + rl) * DM;
    float v[5][8];
    float s = 0.f;
#pragma unroll
    for (int i = 0; i < 5; ++i) {
      f32x4 a = *(const f32x4*)(hsrc + (q + 16 * i) * 8), b = *(const f32x4*)(hsrc + (q + 16 * i) * 8 + 4);
      const v4u xv = *(const v4u*)(x + (size_t)(rb * 64 + rl) * DM + (q + 16 * i) * 8);
      a[0] += bflo(xv.x); a[1] += bfhi(xv.x); a[2] += bflo(xv.y); a[3] += bfhi(xv.y);
      b[0] += bflo(xv.z); b[1] += bfhi(xv.z); b[2] += bflo(xv.w); b[3] += bfhi(xv.w);
#pragma unroll
      for (int j = 0; j < 4; ++j) { v[i][j] = a[j]; v[i][4 + j] = b[j]; s += a[j] + b[j]; }
    }
#pragma unroll
    for (int o = 1; o < 16; o <<= 1) s += __shfl_xor(s, o);
    const float mean = s * (1.f / DM);
    float qq = 0.f;
#pragma unroll
    for (int i = 0; i < 5; ++i)
#pragma unroll
      for (int j = 0; j < 8; ++j) { float d = v[i][j] - mean; qq += d * d; }
#pragma unroll
    for (int o = 1; o < 16; o <<= 1) qq += __shfl_xor(qq, o);
    const float rs = 1.f / sqrtf(qq * (1.f / DM) + 1e-5f);
#pragma unroll
    for (int i = 0; i < 5; ++i) {
      const int pc = q + 16 * i, kk = pc >> 2, g = pc & 3;
      v4u o;
      o.x = pack2((v[i][0] - mean) * rs, (v[i][1] - mean) * rs);
      o.y = pack2((v[i][2] - mean) * rs, (v[i][3] - mean) * rs);
      o.z = pack2((v[i][4] - mean) * rs, (v[i][5] - mean) * rs);
      o.w = pack2((v[i][6] - mean) * rs, (v[i][7] - mean) * rs);
      *(LAS v4u*)(L + ((rl >> 4) * 20 + kk) * 1024 + (g * 16 + (rl & 15)) * 16) = o;
    }
  }
  __syncthreads();
  f32x4 acc[4][4];
#pragma unroll
  for (int i = 0; i < 4; ++i)
#pragma unroll
    for (int m = 0; m < 4; ++m) acc[i][m] = f32x4{0, 0, 0, 0};
  LAS unsigned char* xr = L + lane * 16;
  v4u bq[2][4];
#pragma unroll
  for (int m = 0; m < 4; ++m) bq[0][m] = *(LAS v4u*)(xr + (m * 20) * 1024);
#pragma unroll
  for (int kk = 0; kk < KS; ++kk) {
    if (kk + 1 < KS) {
#pragma unroll
      for (int m = 0; m < 4; ++m) bq[(kk + 1) & 1][m] = *(LAS v4u*)(xr + (m * 20 + kk + 1) * 1024);
    }
    SCHED_FENCE();
#pragma unroll
    for (int i = 0; i < 4; ++i)
#pragma unroll
      for (int m = 0; m < 4; ++m) acc[i][m] = mfma32(wq[kk % 3][i], bq[kk & 1][m], acc[i][m]);
    SCHED_FENCE();
    if (kk + 3 < KS) {
#pragma unroll
      for (int i = 0; i < 4; ++i) wq[kk % 3][i] = *(const v4u*)(wsrc + (i * 20 + kk + 3) * 1024);
    }
    SCHED_FENCE();
  }
#pragma unroll
  for (int m = 0; m < 4; ++m) {
    float o[8];
#pragma unroll
    for (int r = 0; r < 4; ++r) {
      o[r] = gelu_tanh(acc[0][m][r]) * acc[1][m][r];
      o[4 + r] = gelu_tanh(acc[2][m][r]) * acc[3][m][r];
    }
    v4u ov = {pack2(o[0], o[1]), pack2(o[2], o[3]), pack2(o[4], o[5]), pack2(o[6], o[7])};
    *(v4u*)(ws + OFF_AF + ((size_t)(rb * 4 + m) * 48 + fp) * 1024 + lane * 16) = ov;
  }
}

__global__ void __launch_bounds__(512) k_ffn2(const u16* __restrict__ x, unsigned char* __restrict__ ws, u16* __restrict__ out) {
  extern __shared__ __attribute__((aligned(16))) unsigned char smem[];
  LAS unsigned char* L = (LAS unsigned char*)smem;
  const int tid = threadIdx.x, lane = tid & 63, g = lane >> 4, n = lane & 15, w = __builtin_amdgcn_readfirstlane(tid >> 6);
  const int rb = blockIdx.x & 31, cb = blockIdx.x >> 5;
  const unsigned char* wsrc = ws + OFF_WD + ((size_t)(cb * 5) * 48 + 6 * w) * 1024 + lane * 16;
  const unsigned char* xsrc = ws + OFF_AF + ((size_t)(rb * 4) * 48 + 6 * w) * 1024 + lane * 16;
  f32x4 acc[5][4];
#pragma unroll
  for (int i = 0; i < 5; ++i)
#pragma unroll
    for (int m = 0; m < 4; ++m) acc[i][m] = f32x4{0, 0, 0, 0};
  v4u a[3][5], b[3][4];
#pragma unroll
  for (int k = 0; k < 3; ++k) {
#pragma unroll
    for (int i = 0; i < 5; ++i) a[k][i] = *(const v4u*)(wsrc + (i * 48 + k) * 1024);
#pragma unroll
    for (int m = 0; m < 4; ++m) b[k][m] = *(const v4u*)(xsrc + (m * 48 + k) * 1024);
  }
  SCHED_FENCE();
#pragma unroll
  for (int k = 0; k < 6; ++k) {
#pragma unroll
    for (int i = 0; i < 5; ++i)
#pragma unroll
      for (int m = 0; m < 4; ++m) acc[i][m] = mfma32(a[k % 3][i], b[k % 3][m], acc[i][m]);
    SCHED_FENCE();
    if (k + 3 < 6) {
#pragma unroll
      for (int i = 0; i < 5; ++i) a[k % 3][i] = *(const v4u*)(wsrc + (i * 48 + k + 3) * 1024);
#pragma unroll
      for (int m = 0; m < 4; ++m) b[k % 3][m] = *(const v4u*)(xsrc + (m * 48 + k + 3) * 1024);
    }
    SCHED_FENCE();
  }
#pragma unroll
  for (int i = 0; i < 5; ++i)
#pragma unroll
    for (int m = 0; m < 4; ++m) *(LAS f32x4*)(L + (w * 20 + i * 4 + m) * 1024 + lane * 16) = acc[i][m];
  __syncthreads();
  const float* hsrc = (const float*)(ws + OFF_H);
  for (int tl = w; tl < 20; tl += 8) {
    f32x4 s = *(LAS f32x4*)(L + tl * 1024 + lane * 16);
#pragma unroll
    for (int ww = 1; ww < 8; ++ww) s += *(LAS f32x4*)(L + (ww * 20 + tl) * 1024 + lane * 16);
    const int i = tl >> 2, m = tl & 3;
    const int t = (rb * 4 + m) * 16 + n, col = (cb * 5 + i) * 16 + g * 4;
    f32x4 hv = *(const f32x4*)(hsrc + (size_t)t * DM + col);
    const v2u xv = *(const v2u*)(x + (size_t)t * DM + col);
    hv[0] += bflo(xv.x); hv[1] += bfhi(xv.x); hv[2] += bflo(xv.y); hv[3] += bfhi(xv.y);
    v2u o = {pack2(s[0] + hv[0], s[1] + hv[1]), pack2(s[2] + hv[2], s[3] + hv[3])};
    *(v2u*)(out + (size_t)t * DM + col) = o;
  }
}

static void launch_front(void* const* d_in, void* d_ws, hipStream_t st) {
  const u16* x = (const u16*)d_in[0]; const u16* mem0 = (const u16*)d_in[1];
  const u16* Wq = (const u16*)d_in[2]; const u16* Wk = (const u16*)d_in[3]; const u16* Wv = (const u16*)d_in[4]; const u16* Wo = (const u16*)d_in[5];
  const u16* wg = (const u16*)d_in[8]; const u16* wu = (const u16*)d_in[9]; const u16* wd = (const u16*)d_in[10];
  unsigned char* ws = (unsigned char*)d_ws;
  unsigned char* xb = (unsigned char*)(((uintptr_t)ws + (2u << 20) - 1) & ~(uintptr_t)((2u << 20) - 1));
  (void)hipFuncSetAttribute((const void*)k_chain, hipFuncAttributeMaxDynamicSharedMemorySize, CH_LDS);
  k_pre<<<PRE_GRID, 256, 0, st>>>(x, mem0, Wq, Wk, Wv, Wo, wg, wu, wd, ws, xb);
  k_chain<<<CH_GRID, CH_T, CH_LDS, st>>>(ws, xb);
}
static void run(void* const* d_in, void* d_out, void* d_ws, hipStream_t st) {
  const u16* x = (const u16*)d_in[0];
  unsigned char* ws = (unsigned char*)d_ws;
  (void)hipFuncSetAttribute((const void*)k_ffn1, hipFuncAttributeMaxDynamicSharedMemorySize, 81920);
  (void)hipFuncSetAttribute((const void*)k_ffn2, hipFuncAttributeMaxDynamicSharedMemorySize, 163840);
  launch_front(d_in, d_ws, st);
  k_ffn1<<<32 * 6, 512, 81920, st>>>(x, ws);
  k_ffn2<<<256, 512, 163840, st>>>(x, ws, (u16*)d_out);
}
}

extern "C" void kernel_launch(void* const* d_in, const int* in_sizes, int n_in, void* d_out, int out_size, void* d_ws, size_t ws_size,
                              hipStream_t stream) {
  rmt::run(d_in, d_out, d_ws, stream);
}
```
